```python
import torch

def _softmax(x, axis=-1):
    return torch.softmax(x, dim=axis)
def _gelu(x):
    return torch.nn.functional.gelu(x, approximate="tanh")
def _silu(x):
    return torch.nn.functional.silu(x)
def _sigmoid(x):
    return torch.sigmoid(x)
def _layernorm(x, axis=-1, eps=1e-5):
    m = x.mean(dim=axis, keepdim=True)
    v = x.var(dim=axis, keepdim=True, unbiased=False)
    return (x - m) / torch.sqrt(v + eps)
def _scatter_add(tgt, idx, src, axis=0):
    return tgt.clone().index_add_(axis, idx, src)
def _segment_sum(src, seg, n):
    out = torch.zeros((n,) + src.shape[1:], dtype=src.dtype)
    return out.index_add_(0, seg, src)
def _pad(x, pw):
    flat = tuple(v for pair in reversed(pw) for v in pair)
    return torch.nn.functional.pad(x, flat)

kernel_name = 'parallel_sum__spectral_gfnet__glu_gated_reglu__prompt'

def setup_inputs():

    _w = (lambda a: (lambda t: t.to(torch.bfloat16) if t.is_floating_point() else t)(torch.as_tensor(a)))
    def _A_setup():
        B, S, D = 1, 2048, 512
        Df = D // 2 + 1
        return {
            "x": _w((lambda *s: torch.randn(*s, dtype=torch.bfloat16) * 0.05)(B, S, D)),
            "w_r": _w((lambda *s: torch.randn(*s, dtype=torch.bfloat16) * 0.4)(S, Df) * 0.02),
            "w_i": _w((lambda *s: torch.randn(*s, dtype=torch.bfloat16) * 0.4)(S, Df) * 0.02),
        }
    _A_in = _A_setup()

    def _B_setup():
        B, S, D, F = 1, 2048, 512, 1792
        return {
            "x": _w((lambda *s: torch.randn(*s, dtype=torch.bfloat16) * 0.05)(B, S, D)),
            "W_gate": _w((lambda *s: torch.randn(*s, dtype=torch.bfloat16) * 0.2)(D, F) * (2.0 / D) ** 0.5),
            "W_up": _w((lambda *s: torch.randn(*s, dtype=torch.bfloat16) * 0.2)(D, F) * (2.0 / D) ** 0.5),
            "W_down": _w((lambda *s: torch.randn(*s, dtype=torch.bfloat16) * 0.2)(F, D) * (1.0 / F) ** 0.5),
        }
    _B_in = _B_setup()

    return {
        "A_x": _A_in["x"],
        "A_w_r": _A_in["w_r"],
        "A_w_i": _A_in["w_i"],
        "B_x": _B_in["x"],
        "B_W_gate": _B_in["W_gate"],
        "B_W_up": _B_in["W_up"],
        "B_W_down": _B_in["W_down"],
    }

def reference(A_x, A_w_r, A_w_i, B_x, B_W_gate, B_W_up, B_W_down):
    def _A_ref(x, w_r, w_i):
        B, S, D = x.shape
        X = (lambda x, axes=(-2,-1): torch.fft.rfft2(x.float(), dim=axes))(x, axes=(-2, -1))
        W = w_r + 1j * w_i
        X = X * W
        y = (lambda x, s, axes=(-2,-1): torch.fft.irfft2(x, s=s, dim=axes).to(torch.bfloat16))(X, (S, D), axes=(-2, -1))
        return y

    def _B_ref(x, W_gate, W_up, W_down):
        gate = torch.einsum("bsd,df->bsf", x, W_gate)
        up = torch.einsum("bsd,df->bsf", x, W_up)
        gate = gate * (lambda x, dt: x.to(dt))(gate > 0, gate.dtype)
        h = gate * up
        return torch.einsum("bsf,fd->bsd", h, W_down)

    x = A_x
    xn = _layernorm(x, axis=-1)
    return x + _A_ref(xn, A_w_r, A_w_i) + _B_ref(xn, B_W_gate, B_W_up, B_W_down)
```

```cpp
#include <hip/hip_runtime.h>
#include <stdint.h>

typedef __attribute__((ext_vector_type(8))) __bf16 bf16x8;
typedef __attribute__((ext_vector_type(4))) __bf16 bf16x4;
typedef __attribute__((ext_vector_type(2))) __bf16 bf16x2;
typedef __attribute__((ext_vector_type(2))) _Float16 f16x2;
typedef __attribute__((ext_vector_type(16))) float f32x16;
typedef __attribute__((ext_vector_type(4))) float f32x4;
typedef __attribute__((ext_vector_type(2))) float f32x2;
typedef __attribute__((ext_vector_type(4))) uint32_t u32x4;
typedef __attribute__((ext_vector_type(2))) uint32_t u32x2;
#define LDSP __attribute__((address_space(3)))
#define GLBP __attribute__((address_space(1)))

constexpr int S_ = 2048, D_ = 512, F_ = 1792, DF_ = 257;

constexpr size_t WS_H  = 0;
constexpr size_t WS_X1 = WS_H + 64ull * 112 * 1024;
constexpr size_t WS_Z  = WS_X1 + 2097152;
constexpr size_t WS_WD = WS_Z + 2097152;
constexpr size_t WS_WT = WS_WD + 112ull * 16 * 1024;
constexpr size_t WS_UV = WS_WT + 2097152;

constexpr double kPi = 3.14159265358979323846264338327950288;
constexpr double c_cos_small(double x) { double x2 = x * x, t = 1.0, s = 1.0; for (int i = 1; i < 14; ++i) { t *= -x2 / ((2 * i - 1) * (2 * i)); s += t; } return s; }
constexpr double c_sin_small(double x) { double x2 = x * x, t = x, s = x; for (int i = 1; i < 14; ++i) { t *= -x2 / ((2 * i) * (2 * i + 1)); s += t; } return s; }
constexpr double c_cos2pi(int n, int N) {
    n %= N; if (2 * n > N) n = N - n;
    bool neg = false;
    if (4 * n > N) { n = N / 2 - n; neg = true; }
    double r = (8 * n <= N) ? c_cos_small(2 * kPi * n / N) : c_sin_small(2 * kPi * (N / 4 - n) / N);
    return neg ? -r : r;
}
constexpr uint16_t c_f2bf(double v) { float f = (float)v; uint32_t u = __builtin_bit_cast(uint32_t, f); u += 0x7fff + ((u >> 16) & 1); return (uint16_t)(u >> 16); }
struct Pair512 { uint32_t v[512]; };
constexpr Pair512 make_pair(double scale) {
    Pair512 t{};
    for (int i = 0; i < 512; ++i) t.v[i] = (uint32_t)c_f2bf(scale * c_cos2pi(i, 512)) | ((uint32_t)c_f2bf(scale * c_cos2pi(i + 128, 512)) << 16);
    return t;
}
constexpr Pair512 kPairF = make_pair(1.0);
constexpr Pair512 kPairG = make_pair(1.0 / 524288.0);
struct Tab { uint64_t v[512 * 128]; };
constexpr Tab make_tabF() {
    Tab t{};
    for (int d = 0; d < 512; ++d)
        for (int g = 0; g < 128; ++g) {
            const int k = 16 * (g >> 3) + 8 * ((g >> 2) & 1) + 4 * (g & 1) + 2 * ((g >> 1) & 1);
            t.v[d * 128 + g] = (uint64_t)(k == 0 ? (0x3f80u | ((d & 1) ? 0xbf800000u : 0x3f800000u)) : kPairF.v[(d * k) & 511]) | ((uint64_t)kPairF.v[(d * (k + 1)) & 511] << 32);
        }
    return t;
}
constexpr Tab make_tabG() {
    Tab t{};
    for (int i = 0; i < 32 * 16 * 64; ++i) {
        const int d = 32 * ((i >> 6) & 15) + (i & 31), k = 8 * (i >> 10) + 4 * ((i >> 5) & 1);
        t.v[2 * i] = (uint64_t)(k == 0 ? (0x3580u | ((d & 1) ? 0xb5800000u : 0x35800000u)) : kPairG.v[(d * k) & 511]) | ((uint64_t)kPairG.v[(d * (k + 1)) & 511] << 32);
        t.v[2 * i + 1] = (uint64_t)kPairG.v[(d * (k + 2)) & 511] | ((uint64_t)kPairG.v[(d * (k + 3)) & 511] << 32);
    }
    return t;
}
__device__ __attribute__((aligned(1024))) constexpr Tab kTabF = make_tabF();
__device__ __attribute__((aligned(1024))) constexpr Tab kTabG = make_tabG();

__device__ __forceinline__ bf16x2 as_bf2(uint32_t v) { return __builtin_bit_cast(bf16x2, v); }
__device__ __forceinline__ float bf_lo(uint32_t v) { return __builtin_bit_cast(float, v << 16); }
__device__ __forceinline__ float bf_hi(uint32_t v) { return __builtin_bit_cast(float, v & 0xffff0000u); }
__device__ __forceinline__ uint32_t pk_bf16(float a, float b) { f32x2 v = {a, b}; return __builtin_bit_cast(uint32_t, __builtin_convertvector(v, bf16x2)); }
__device__ __forceinline__ float rbf(float a) { return bf_lo(pk_bf16(a, 0.f) ); }
__device__ __forceinline__ uint32_t pk_f16(float a, float b) { f32x2 v = {a, b}; return __builtin_bit_cast(uint32_t, __builtin_convertvector(v, f16x2)); }
template <int CTRL> __device__ __forceinline__ float dppf(float v) {
    return __builtin_bit_cast(float, __builtin_amdgcn_update_dpp(0, __builtin_bit_cast(int, v), CTRL, 0xf, 0xf, true));
}
#define MFMA32(a, b, c) __builtin_amdgcn_mfma_f32_32x32x16_bf16((a), (b), (c), 0, 0, 0)

constexpr int K1_WRING = 0, K1_ARING = 98304, K1_RINV = 131072, K1_LDS = 131072 + 512;

__global__ __launch_bounds__(512, 2) void k1_kernel(const uint16_t* __restrict__ x, const uint16_t* __restrict__ fr, const uint16_t* __restrict__ fi,
                                                    const uint16_t* __restrict__ wg, const uint16_t* __restrict__ wu, const uint16_t* __restrict__ wd,
                                                    char* __restrict__ ws) {
    __shared__ __attribute__((aligned(1024))) char lds[K1_LDS];
    const int tid = threadIdx.x, l = tid & 63;
    const int w = __builtin_amdgcn_readfirstlane(tid >> 6);
    const int b = blockIdx.x;
    const int xcd = b & 7, idx = b >> 3;
    const int mt = 8 * (xcd & 1) + (idx & 7), nt = 4 * (xcd >> 1) + (idx >> 3);
    const bool dft = nt >= 14;
    const int hh = l >> 5, l31 = l & 31;

    uint32_t auxT[8];
    {
        const int tp = (b >> 2) * 4 + (w & 3), k = 64 * (b & 3) + l;
        const int v = tp & 3, m = ((v & 1) << 1) | (v >> 1);
        const int j0 = (tp >> 5) + 8 * ((tp >> 2) & 7) + 512 * m + 256 * (w >> 2);
#pragma unroll
        for (int r = 0; r < 4; ++r) {
            auxT[2 * r] = fr[(j0 + 64 * r) * DF_ + k];
            auxT[2 * r + 1] = fi[(j0 + 64 * r) * DF_ + k];
        }
    }
    uint32_t auxD[8];
    if (w < 7) {
        const int id = b * 7 + w, ks = id >> 4, dt = id & 15;
#pragma unroll
        for (int j = 0; j < 8; ++j) auxD[j] = wd[(16 * ks + 8 * (j >> 2) + 4 * hh + (j & 3)) * D_ + 32 * dt + l31];
    } else {
        const int v = b & 3, m = ((v & 1) << 1) | (v >> 1);
        const int j = (b >> 5) + 8 * ((b >> 2) & 7) + 512 * m + 64 * (l & 7), nj = (2048 - j) & 2047;
        auxD[0] = fr[j * DF_]; auxD[1] = fi[j * DF_]; auxD[2] = fr[nj * DF_]; auxD[3] = fi[nj * DF_];
        auxD[4] = fr[j * DF_ + 256]; auxD[5] = fi[j * DF_ + 256]; auxD[6] = fr[nj * DF_ + 256]; auxD[7] = fi[nj * DF_ + 256];
    }

    u32x4 xr[2][8];
    {
        const char* xb = (const char*)x + (size_t)(128 * mt + 16 * w) * 1024;
        const uint32_t xo = (uint32_t)((l >> 3) * 1024 + (l & 7) * 16), xo1 = xo + 8192;
#pragma unroll
        for (int j = 0; j < 8; ++j) {
            asm volatile("global_load_dwordx4 %0, %1, %2 offset:%3" : "=v"(xr[0][j]) : "v"(xo), "s"(xb), "i"(j * 128));
            asm volatile("global_load_dwordx4 %0, %1, %2 offset:%3" : "=v"(xr[1][j]) : "v"(xo1), "s"(xb), "i"(j * 128));
        }
    }
    const int cp = w & 3;
    const int pitch = dft ? 1024 : 3584;
    const char* wbase;
    if (dft) wbase = (const char*)kTabF.v + 512 * (nt - 14) + 128 * cp;
    else wbase = (const char*)((cp >> 1) ? wu : wg) + 256 * nt + 128 * (cp & 1);
    wbase += (size_t)(32 * (w >> 2)) * pitch;
    const uint32_t woff = (uint32_t)(((l >> 2) & 7) * pitch + 64 * hh + 16 * (l & 3));
    auto stageW = [&](int t) {
        const int slot = t % 3;
#pragma unroll
        for (int i = 0; i < 4; ++i) {
            const char* src = wbase + (size_t)(64 * t + 8 * i) * pitch;
            __builtin_amdgcn_global_load_lds((const GLBP void*)(src + woff), (LDSP void*)(lds + K1_WRING + slot * 32768 + (4 * (w >> 2) + i) * 4096 + cp * 1024), 16, 0, 0);
        }
    };
    stageW(0); stageW(1); stageW(2);

    asm volatile("s_waitcnt vmcnt(12)"
                 : "+v"(xr[0][0]), "+v"(xr[0][1]), "+v"(xr[0][2]), "+v"(xr[0][3]), "+v"(xr[0][4]), "+v"(xr[0][5]), "+v"(xr[0][6]), "+v"(xr[0][7]),
                   "+v"(xr[1][0]), "+v"(xr[1][1]), "+v"(xr[1][2]), "+v"(xr[1][3]), "+v"(xr[1][4]), "+v"(xr[1][5]), "+v"(xr[1][6]), "+v"(xr[1][7]));
    float mu[2], mb[2], esq[2];
    const bf16x2 ones = {(__bf16)1.0f, (__bf16)1.0f};
#pragma unroll
    for (int g = 0; g < 2; ++g) {
        float s0 = 0.f, s1 = 0.f;
#pragma unroll
        for (int j = 0; j < 8; ++j) {
            s0 = __builtin_amdgcn_fdot2_f32_bf16(as_bf2(xr[g][j][0]), ones, s0, false);
            s1 = __builtin_amdgcn_fdot2_f32_bf16(as_bf2(xr[g][j][1]), ones, s1, false);
            s0 = __builtin_amdgcn_fdot2_f32_bf16(as_bf2(xr[g][j][2]), ones, s0, false);
            s1 = __builtin_amdgcn_fdot2_f32_bf16(as_bf2(xr[g][j][3]), ones, s1, false);
        }
        float s = s0 + s1;
        s += dppf<0xB1>(s); s += dppf<0x4E>(s); s += dppf<0x141>(s);
        mu[g] = s * (1.0f / 512.0f);
        mb[g] = rbf(mu[g]);
        esq[g] = 0.f;
    }
    const int arow = 16 * w + (l >> 3);
    const uint32_t awr0 = K1_ARING + arow * 128 + (((l & 7) ^ ((arow >> 1) & 7)) << 4);
    const uint32_t awr1 = K1_ARING + (arow + 8) * 128 + (((l & 7) ^ (((arow + 8) >> 1) & 7)) << 4);
    auto convertA = [&](int j) {
#pragma unroll
        for (int g = 0; g < 2; ++g) {
            u32x4 o;
#pragma unroll
            for (int e = 0; e < 4; ++e) {
                const float lo = bf_lo(xr[g][j][e]) - mb[g], hi = bf_hi(xr[g][j][e]) - mb[g];
                esq[g] = __builtin_fmaf(lo, lo, esq[g]);
                esq[g] = __builtin_fmaf(hi, hi, esq[g]);
                o[e] = pk_bf16(lo, hi);
            }
            *(u32x4*)(lds + (g ? awr1 : awr0) + (j & 1) * 16384) = o;
        }
    };
    convertA(0);

    const int wsd = w & 1, wn = w >> 1;
    const uint32_t brd = K1_WRING + hh * 4096 + ((l >> 2) & 3) * 64 + ((l >> 4) & 1) * 32 + (l & 3) * 8 + wn * 512;
    const uint32_t ard = K1_ARING + (64 * wsd + l31) * 128 + ((hh ^ ((l31 >> 1) & 7)) << 4);
    f32x16 acc[2][2];
#pragma unroll
    for (int i = 0; i < 2; ++i)
#pragma unroll
        for (int j = 0; j < 2; ++j)
#pragma unroll
            for (int e = 0; e < 16; ++e) acc[i][j][e] = 0.f;

#pragma unroll
    for (int t = 0; t < 8; ++t) {
        if (t == 0) asm volatile("s_waitcnt vmcnt(8) lgkmcnt(0)" ::: "memory");
        else if (t < 7) asm volatile("s_waitcnt vmcnt(4) lgkmcnt(0)" ::: "memory");
        else asm volatile("s_waitcnt vmcnt(0) lgkmcnt(0)" ::: "memory");
        __builtin_amdgcn_s_barrier();
        asm volatile("" ::: "memory");
        if (t >= 1 && t + 2 < 8) stageW(t + 2);
        if (t + 1 < 8) convertA(t + 1);
        if (t == 6) {
#pragma unroll
            for (int g = 0; g < 2; ++g) {
                float s = esq[g];
                s += dppf<0xB1>(s); s += dppf<0x4E>(s); s += dppf<0x141>(s);
                const float dm = mu[g] - mb[g];
                float var = s * (1.0f / 512.0f) - dm * dm;
                var = var > 0.f ? var : 0.f;
                const float rr = rbf(__builtin_sqrtf(rbf(rbf(var) + 1e-5f)));
                if ((l & 7) == 0) *(float*)(lds + K1_RINV + (arow + 8 * g) * 4) = 1.0f / rr;
            }
        }
        const char* wslot = lds + (t % 3) * 32768;
        const char* aslot = lds + (t & 1) * 16384;
#pragma unroll
        for (int ks = 0; ks < 4; ++ks) {
            bf16x8 bfr[2], afr[2];
#pragma unroll
            for (int gu = 0; gu < 2; ++gu) {
                bf16x4 p0 = __builtin_amdgcn_ds_read_tr16_b64_v4bf16((LDSP bf16x4*)(wslot + brd + ks * 8192 + gu * 2048));
                bf16x4 p1 = __builtin_amdgcn_ds_read_tr16_b64_v4bf16((LDSP bf16x4*)(wslot + brd + ks * 8192 + gu * 2048 + 256));
                bfr[gu] = __builtin_shufflevector(p0, p1, 0, 1, 2, 3, 4, 5, 6, 7);
            }
#pragma unroll
            for (int st = 0; st < 2; ++st) afr[st] = *(const bf16x8*)(aslot + (ard ^ (ks * 32)) + st * 4096);
#pragma unroll
            for (int gu = 0; gu < 2; ++gu)
#pragma unroll
                for (int st = 0; st < 2; ++st) acc[gu][st] = MFMA32(bfr[gu], afr[st], acc[gu][st]);
        }
    }

#pragma unroll
    for (int st = 0; st < 2; ++st) {
        const float ri = *(const float*)(lds + K1_RINV + (64 * wsd + 32 * st + l31) * 4);
        if (!dft) {
            char* hp = ws + WS_H + ((size_t)(4 * mt + 2 * wsd + st) * 112 + 8 * nt + 2 * wn) * 1024 + l * 16;
#pragma unroll
            for (int u = 0; u < 2; ++u) {
                u32x4 o;
#pragma unroll
                for (int e = 0; e < 4; ++e) {
                    const uint32_t gp = pk_bf16(acc[0][st][8 * u + 2 * e] * ri, acc[0][st][8 * u + 2 * e + 1] * ri);
                    const uint32_t up = pk_bf16(acc[1][st][8 * u + 2 * e] * ri, acc[1][st][8 * u + 2 * e + 1] * ri);
                    const float g0 = __builtin_fmaxf(bf_lo(gp), 0.f), g1 = __builtin_fmaxf(bf_hi(gp), 0.f);
                    o[e] = pk_bf16(g0 * bf_lo(up), g1 * bf_hi(up));
                }
                *(u32x4*)(hp + u * 1024) = o;
            }
        } else {
            const int s = 128 * mt + 64 * wsd + 32 * st + l31;
#pragma unroll
            for (int gu = 0; gu < 2; ++gu)
#pragma unroll
                for (int u = 0; u < 2; ++u) {
                    const int kg = 4 * (8 * (nt - 14) + 4 * gu + wn) + 2 * u + hh;
                    u32x4 o;
#pragma unroll
                    for (int e = 0; e < 4; ++e) o[e] = pk_f16(acc[gu][st][8 * u + 2 * e] * ri, acc[gu][st][8 * u + 2 * e + 1] * ri);
                    *(u32x4*)(ws + WS_X1 + ((size_t)kg * 2048 + s) * 16) = o;
                }
        }
    }
    {
        const int tp = (b >> 2) * 4 + (w & 3), k = 64 * (b & 3) + l;
        u32x4 o;
#pragma unroll
        for (int r = 0; r < 4; ++r) o[r] = auxT[2 * r] | (auxT[2 * r + 1] << 16);
        *(u32x4*)(ws + WS_WT + ((size_t)k * 256 + tp) * 32 + 16 * (w >> 2)) = o;
    }
    if (w < 7) {
        u32x4 o;
#pragma unroll
        for (int e = 0; e < 4; ++e) o[e] = auxD[2 * e] | (auxD[2 * e + 1] << 16);
        *(u32x4*)(ws + WS_WD + (size_t)(b * 7 + w) * 1024 + l * 16) = o;
    } else if (l < 8) {
        const int v = b & 3, m = ((v & 1) << 1) | (v >> 1);
        const int j = (b >> 5) + 8 * ((b >> 2) & 7) + 512 * m + 64 * (l & 7), nj = (2048 - j) & 2047;
        const float pr = 0.5f * (bf_lo(auxD[0]) + bf_lo(auxD[2])), pi = 0.5f * (bf_lo(auxD[1]) - bf_lo(auxD[3]));
        const float qr = 0.5f * (bf_lo(auxD[4]) + bf_lo(auxD[6])), qi = 0.5f * (bf_lo(auxD[5]) - bf_lo(auxD[7]));
        float vr = 0.5f * (pr - qr), vi = 0.5f * (pi - qi);
        const int m1 = j >> 9, m2 = nj >> 9;
        const int e = ((m1 == 0 ? 0 : m1 == 3 ? 1 : 2) + (m2 == 0 ? 0 : m2 == 3 ? 1 : 2)) & 3;
        float tr = vr, ti = vi;
        if (e == 1) { tr = -vi; ti = vr; } else if (e == 2) { tr = -vr; ti = -vi; } else if (e == 3) { tr = vi; ti = -vr; }
        f32x4 o = {0.5f * (pr + qr), 0.5f * (pi + qi), tr, ti};
        *(f32x4*)(ws + WS_UV + (size_t)(b * 8 + l) * 16) = o;
    }
}

__device__ __forceinline__ f32x2 cmul(f32x2 a, f32x2 b) { return a.xx * b + a.yy * (f32x2){-b.y, b.x}; }
__device__ __forceinline__ f32x2 cmulc(f32x2 a, f32x2 b) { return a.xx * (f32x2){b.x, -b.y} + a.yy * b.yx; }
template <int SG> __device__ __forceinline__ f32x2 rot90(f32x2 a) { return SG > 0 ? (f32x2){-a.y, a.x} : (f32x2){a.y, -a.x}; }
template <int SG> __device__ __forceinline__ void dft8(f32x2 (&x)[8]) {
    const float h = 0.70710678118654752440f;
    f32x2 t0 = x[0] + x[4], t4 = x[0] - x[4], t1 = x[1] + x[5], t5 = x[1] - x[5];
    f32x2 t2 = x[2] + x[6], t6 = x[2] - x[6], t3 = x[3] + x[7], t7 = x[3] - x[7];
    t5 = (t5 + rot90<SG>(t5)) * h;
    t6 = rot90<SG>(t6);
    t7 = (rot90<SG>(t7) - t7) * h;
    f32x2 s0 = t0 + t2, s1 = t0 - t2, s2 = t1 + t3, s3 = rot90<SG>(t1 - t3);
    x[0] = s0 + s2; x[4] = s0 - s2; x[2] = s1 + s3; x[6] = s1 - s3;
    s0 = t4 + t6; s1 = t4 - t6; s2 = t5 + t7; s3 = rot90<SG>(t5 - t7);
    x[1] = s0 + s2; x[5] = s0 - s2; x[3] = s1 + s3; x[7] = s1 - s3;
}
__device__ __forceinline__ f32x2 twid(int n, float inv) {
    const float a = (float)n * inv;
    return (f32x2){__builtin_amdgcn_cosf(a), -__builtin_amdgcn_sinf(a)};
}

__global__ __launch_bounds__(256) void k2_kernel(char* __restrict__ ws) {
    __shared__ __attribute__((aligned(16))) f32x2 buf[8 * 288];
    const int t = threadIdx.x, b = blockIdx.x;
    const int k = (b & 7) * 32 + (b >> 3);
    uint32_t xin[8];
    {
        const char* xp = ws + WS_X1 + (size_t)(k >> 2) * 32768 + (k & 3) * 4 + t * 16;
#pragma unroll
        for (int a = 0; a < 8; ++a) xin[a] = *(const uint32_t*)(xp + a * 4096);
    }
    u32x4 wt[2];
    wt[0] = *(const u32x4*)(ws + WS_WT + ((size_t)k * 256 + t) * 32);
    wt[1] = *(const u32x4*)(ws + WS_WT + ((size_t)k * 256 + t) * 32 + 16);
    const int p = t >> 5, u = t & 31, q = (t >> 2) & 7, v = t & 3;
    f32x2 twA[8], twB[8], twC[8];
#pragma unroll
    for (int i = 1; i < 8; ++i) { twA[i] = twid(t * i, 1.0f / 2048.0f); twB[i] = twid(u * i, 1.0f / 256.0f); twC[i] = twid(v * i, 1.0f / 32.0f); }
    const float sg = v < 2 ? 1.f : -1.f, al = v == 0 ? 1.f : v == 1 ? -1.f : 0.f, be = v < 2 ? 0.f : 1.f;
    f32x2 x[8];
#pragma unroll
    for (int a = 0; a < 8; ++a) x[a] = __builtin_convertvector(__builtin_bit_cast(f16x2, xin[a]), f32x2);
    dft8<-1>(x);
#pragma unroll
    for (int i = 1; i < 8; ++i) x[i] = cmul(x[i], twA[i]);
#pragma unroll
    for (int i = 0; i < 8; ++i) buf[i * 288 + t] = x[i];
    __syncthreads();
#pragma unroll
    for (int i = 0; i < 8; ++i) x[i] = buf[p * 288 + u + 32 * i];
    dft8<-1>(x);
#pragma unroll
    for (int i = 1; i < 8; ++i) x[i] = cmul(x[i], twB[i]);
    __builtin_amdgcn_wave_barrier();
#pragma unroll
    for (int i = 0; i < 8; ++i) buf[p * 288 + i * 36 + u] = x[i];
    __builtin_amdgcn_wave_barrier();
#pragma unroll
    for (int i = 0; i < 8; ++i) x[i] = buf[p * 288 + q * 36 + v + 4 * i];
    dft8<-1>(x);
#pragma unroll
    for (int i = 1; i < 8; ++i) x[i] = cmul(x[i], twC[i]);
#pragma unroll
    for (int i = 0; i < 8; ++i) {
        f32x2 a = x[i];
        a.x += sg * dppf<0x4E>(x[i].x); a.y += sg * dppf<0x4E>(x[i].y);
        const float px = dppf<0xB1>(a.x), py = dppf<0xB1>(a.y);
        x[i].x = a.x + al * px + be * py; x[i].y = a.y + al * py - be * px;
    }
    if (k != 0) {
#pragma unroll
        for (int i = 0; i < 8; ++i) { const uint32_t wv = wt[i >> 2][i & 3]; x[i] = cmul(x[i], (f32x2){bf_lo(wv), bf_hi(wv)}); }
    } else {
        const int m = ((v & 1) << 1) | (v >> 1), j0 = p + 8 * q + 512 * m;
        __syncthreads();
#pragma unroll
        for (int i = 0; i < 8; ++i) buf[j0 + 64 * i] = x[i];
        __syncthreads();
#pragma unroll
        for (int i = 0; i < 8; ++i) {
            const f32x2 o = buf[(2048 - (j0 + 64 * i)) & 2047];
            const f32x4 uv = *(const f32x4*)(ws + WS_UV + (size_t)(t * 8 + i) * 16);
            x[i] = cmul(x[i], uv.xy) + cmulc(uv.zw, o);
        }
        __syncthreads();
    }
#pragma unroll
    for (int i = 0; i < 8; ++i) {
        const float px = dppf<0xB1>(x[i].x), py = dppf<0xB1>(x[i].y);
        f32x2 a;
        a.x = x[i].x - al * px - be * py; a.y = x[i].y - al * py + be * px;
        x[i].x = a.x - sg * dppf<0x4E>(a.x); x[i].y = a.y - sg * dppf<0x4E>(a.y);
    }
#pragma unroll
    for (int i = 1; i < 8; ++i) x[i] = cmulc(x[i], twC[i]);
    dft8<1>(x);
    __builtin_amdgcn_wave_barrier();
#pragma unroll
    for (int i = 0; i < 8; ++i) buf[p * 288 + q * 36 + v + 4 * i] = x[i];
    __builtin_amdgcn_wave_barrier();
#pragma unroll
    for (int i = 0; i < 8; ++i) x[i] = buf[p * 288 + i * 36 + u];
#pragma unroll
    for (int i = 1; i < 8; ++i) x[i] = cmulc(x[i], twB[i]);
    dft8<1>(x);
    __builtin_amdgcn_wave_barrier();
#pragma unroll
    for (int i = 0; i < 8; ++i) buf[p * 288 + u + 32 * i] = x[i];
    __syncthreads();
#pragma unroll
    for (int i = 0; i < 8; ++i) x[i] = buf[i * 288 + t];
#pragma unroll
    for (int i = 1; i < 8; ++i) x[i] = cmulc(x[i], twA[i]);
    dft8<1>(x);
    {
        char* zp = ws + WS_Z + (size_t)(t >> 5) * 32768 + (k >> 2) * 512 + (t & 31) * 16 + (k & 3) * 4;
#pragma unroll
        for (int a = 0; a < 8; ++a) *(uint32_t*)(zp + (size_t)a * 8 * 32768) = pk_bf16(x[a].x, x[a].y);
    }
}

constexpr int K3_R = 12;
__global__ __launch_bounds__(256, 1) void k3_kernel(const uint16_t* __restrict__ x, const char* __restrict__ ws, uint16_t* __restrict__ out) {
    __shared__ __attribute__((aligned(16))) f32x4 red[4 * 4 * 4 * 64];
    const int tid = threadIdx.x, l = tid & 63;
    const int w = __builtin_amdgcn_readfirstlane(tid >> 6);
    const int b = blockIdx.x, xcd = b & 7, idx = b >> 3;
    const int sp = 8 * (xcd >> 1) + (idx & 7), dp = 4 * (xcd & 1) + (idx >> 3);
    const int hh = l >> 5, l31 = l & 31;
    const char* hB = ws + WS_H + ((size_t)(2 * sp) * 112 + 28 * w) * 1024 + l * 16;
    const char* wA = ws + WS_WD + ((size_t)(28 * w) * 16 + 2 * dp) * 1024 + l * 16;
    const char* zB = ws + WS_Z + ((size_t)(2 * sp) * 32 + 8 * w) * 1024 + l * 16;
    const char* gA = (const char*)kTabG.v + ((size_t)(8 * w) * 16 + 2 * dp) * 1024 + l * 16;
    bf16x8 ring[K3_R][4];
    auto load = [&](int i, bf16x8 (&r)[4]) {
        if (i < 28) {
            r[0] = *(const bf16x8*)(wA + (size_t)i * 16384); r[1] = *(const bf16x8*)(wA + (size_t)i * 16384 + 1024);
            r[2] = *(const bf16x8*)(hB + (size_t)i * 1024); r[3] = *(const bf16x8*)(hB + (size_t)i * 1024 + 112 * 1024);
        } else {
            const int j = i - 28;
            r[0] = *(const bf16x8*)(gA + (size_t)j * 16384); r[1] = *(const bf16x8*)(gA + (size_t)j * 16384 + 1024);
            r[2] = *(const bf16x8*)(zB + (size_t)j * 1024); r[3] = *(const bf16x8*)(zB + (size_t)j * 1024 + 32768);
        }
    };
    const int odt = w >> 1, ost = w & 1;
    const size_t orow = (size_t)(64 * sp + 32 * ost + l31) * D_ + 64 * dp + 32 * odt + 4 * hh;
    u32x2 xres[4];
#pragma unroll
    for (int g = 0; g < 4; ++g) xres[g] = *(const u32x2*)(x + orow + 8 * g);
#pragma unroll
    for (int i = 0; i < K3_R; ++i) load(i, ring[i]);
    f32x16 acc[2][2];
#pragma unroll
    for (int i = 0; i < 2; ++i)
#pragma unroll
        for (int j = 0; j < 2; ++j)
#pragma unroll
            for (int e = 0; e < 16; ++e) acc[i][j][e] = 0.f;
#pragma unroll
    for (int i = 0; i < 36; ++i) {
        bf16x8 (&r)[4] = ring[i % K3_R];
#pragma unroll
        for (int dt = 0; dt < 2; ++dt)
#pragma unroll
            for (int st = 0; st < 2; ++st) acc[dt][st] = MFMA32(r[dt], r[2 + st], acc[dt][st]);
        if (i + K3_R < 36) load(i + K3_R, r);
    }
#pragma unroll
    for (int qd = 0; qd < 4; ++qd)
#pragma unroll
        for (int g = 0; g < 4; ++g) {
            const f32x16& a = acc[qd >> 1][qd & 1];
            red[((qd * 4 + w) * 4 + g) * 64 + l] = (f32x4){a[4 * g], a[4 * g + 1], a[4 * g + 2], a[4 * g + 3]};
        }
    __syncthreads();
#pragma unroll
    for (int g = 0; g < 4; ++g) {
        f32x4 s = red[((w * 4 + 0) * 4 + g) * 64 + l];
#pragma unroll
        for (int o = 1; o < 4; ++o) s += red[((w * 4 + o) * 4 + g) * 64 + l];
        u32x2 o2;
        o2[0] = pk_bf16(bf_lo(xres[g][0]) + s[0], bf_hi(xres[g][0]) + s[1]);
        o2[1] = pk_bf16(bf_lo(xres[g][1]) + s[2], bf_hi(xres[g][1]) + s[3]);
        *(u32x2*)(out + orow + 8 * g) = o2;
    }
}

extern "C" void kernel_launch(void* const* d_in, const int* in_sizes, int n_in, void* d_out, int out_size, void* d_ws, size_t ws_size, hipStream_t stream) {
    const uint16_t* x = (const uint16_t*)d_in[0];
    const uint16_t* fr = (const uint16_t*)d_in[1];
    const uint16_t* fi = (const uint16_t*)d_in[2];
    const uint16_t* wg = (const uint16_t*)d_in[4];
    const uint16_t* wu = (const uint16_t*)d_in[5];
    const uint16_t* wd = (const uint16_t*)d_in[6];
    char* ws = (char*)d_ws;
    k1_kernel<<<256, 512, 0, stream>>>(x, fr, fi, wg, wu, wd, ws);
    k2_kernel<<<256, 256, 0, stream>>>(ws);
    k3_kernel<<<256, 256, 0, stream>>>(x, ws, (uint16_t*)d_out);
}
```

```cpp
#define CODE_LINES 318
#define CODE_SPLIT 32
#define ZFS 32
#define ZGO 18
#define HDEPTH 6
#define POLL_SLEEP 4
#define SCALAR_EPI 1
#define RPRE 4
#define LAG 2
#define ILV 1
#define MIDT 1
#define PIECES 1
#include <hip/hip_runtime.h>
#include <stdint.h>

typedef __attribute__((ext_vector_type(8))) __bf16 bf16x8;
typedef __attribute__((ext_vector_type(4))) __bf16 bf16x4;
typedef __attribute__((ext_vector_type(2))) __bf16 bf16x2;
typedef __attribute__((ext_vector_type(2))) _Float16 f16x2;
typedef __attribute__((ext_vector_type(16))) float f32x16;
typedef __attribute__((ext_vector_type(4))) float f32x4;
typedef __attribute__((ext_vector_type(2))) float f32x2;
typedef __attribute__((ext_vector_type(4))) uint32_t u32x4;
typedef __attribute__((ext_vector_type(2))) uint32_t u32x2;
#define LDSP __attribute__((address_space(3)))

#define TS(i) do {} while (0)
#define TS_FLUSH(kid) do {} while (0)

constexpr int DF_ = 257;

constexpr uint32_t WS_H  = 0;
constexpr uint32_t WS_E  = WS_H + 64u * 112 * 1024;
constexpr uint32_t WS_Z  = WS_E + 2097152;
constexpr uint32_t WS_WT = WS_Z + 2097152;
constexpr uint32_t WS_ST = WS_WT + 257u * 8192;
constexpr uint32_t WS_CS = WS_ST + 16384;
constexpr uint32_t WS_FW = WS_CS + 28672;
constexpr uint32_t WS_FA = WS_FW + 8192;
constexpr uint32_t WS_FH = WS_FA + 2048;
constexpr uint32_t WS_FZ = WS_FH + 2048 * 128;
constexpr uint32_t WS_EP = WS_FZ + 1024 * 128;

__device__ __forceinline__ bf16x2 as_bf2(uint32_t v) { return __builtin_bit_cast(bf16x2, v); }
__device__ __forceinline__ float bf_lo(uint32_t v) { return __builtin_bit_cast(float, v << 16); }
__device__ __forceinline__ float bf_hi(uint32_t v) { return __builtin_bit_cast(float, v & 0xffff0000u); }
__device__ __forceinline__ uint32_t pk_bf16(float a, float b) { f32x2 v = {a, b}; return __builtin_bit_cast(uint32_t, __builtin_convertvector(v, bf16x2)); }
__device__ __forceinline__ float rbf(float a) { return bf_lo(pk_bf16(a, 0.f)); }
__device__ __forceinline__ uint32_t pk_f16(float a, float b) { f32x2 v = {a, b}; return __builtin_bit_cast(uint32_t, __builtin_convertvector(v, f16x2)); }
template <int CTRL> __device__ __forceinline__ float dppf(float v) {
    return __builtin_bit_cast(float, __builtin_amdgcn_update_dpp(0, __builtin_bit_cast(int, v), CTRL, 0xf, 0xf, true));
}
__device__ __forceinline__ float dot2u(uint32_t a, uint32_t b, float c) { return __builtin_amdgcn_fdot2_f32_bf16(as_bf2(a), as_bf2(b), c, false); }
__device__ __forceinline__ f32x16 mfma32(u32x4 a, u32x4 b, f32x16 c) {
    return __builtin_amdgcn_mfma_f32_32x32x16_bf16(__builtin_bit_cast(bf16x8, a), __builtin_bit_cast(bf16x8, b), c, 0, 0, 0);
}
__device__ __forceinline__ void st16_wt(const void* base, uint32_t off, u32x4 v) {
    asm volatile("global_store_dwordx4 %0, %1, %2 sc1\n\ts_nop 1" :: "v"(off), "v"(v), "s"(base) : "memory");
}
__device__ __forceinline__ void st4_wt(const void* base, uint32_t off, uint32_t v) {
    asm volatile("global_store_dword %0, %1, %2 sc1" :: "v"(off), "v"(v), "s"(base) : "memory");
}
__device__ __forceinline__ u32x4 lds_tr2(const char* lds, uint32_t a) {
    bf16x4 p0 = __builtin_amdgcn_ds_read_tr16_b64_v4bf16((LDSP bf16x4*)(lds + a));
    bf16x4 p1 = __builtin_amdgcn_ds_read_tr16_b64_v4bf16((LDSP bf16x4*)(lds + a + 256));
    return __builtin_bit_cast(u32x4, __builtin_shufflevector(p0, p1, 0, 1, 2, 3, 4, 5, 6, 7));
}
__device__ __forceinline__ void glds1(uint32_t dst, uint32_t voff, const void* base) {
    uint32_t keep;
    asm volatile("s_mov_b32 %0, m0\n\ts_mov_b32 m0, %1\n\ts_nop 0\n\tglobal_load_lds_dwordx4 %2, %3\n\ts_mov_b32 m0, %0"
                 : "=&s"(keep) : "s"(dst), "v"(voff), "s"(base) : "memory");
}
__device__ __forceinline__ void ln_finish(float mu, float var, float& mb, float& rinv) {
    var = var > 0.f ? var : 0.f;
    mb = rbf(mu);
    rinv = __builtin_amdgcn_rcpf(rbf(__builtin_amdgcn_sqrtf(rbf(rbf(var) + 1e-5f))));
}

#define WAITV(n) asm volatile("s_waitcnt vmcnt(" #n ")" ::: "memory")
#define BARRIER() do { asm volatile("s_waitcnt lgkmcnt(0)" ::: "memory"); __builtin_amdgcn_s_barrier(); asm volatile("" ::: "memory"); } while (0)
__device__ __forceinline__ void ld16_dev(u32x4& r, const void* base, uint32_t off) { asm volatile("global_load_dwordx4 %0, %1, %2 sc1" : "=v"(r) : "v"(off), "s"(base) : "memory"); }
__device__ __forceinline__ void ld8_dev(u32x2& r, const void* base, uint32_t off) { asm volatile("global_load_dwordx2 %0, %1, %2 sc1" : "=v"(r) : "v"(off), "s"(base) : "memory"); }
__device__ __forceinline__ void ld4_dev(uint32_t& r, const void* base, uint32_t off) { asm volatile("global_load_dword %0, %1, %2 sc1" : "=v"(r) : "v"(off), "s"(base) : "memory"); }
__device__ __forceinline__ void ld16(u32x4& r, const void* base, uint32_t off) { asm volatile("global_load_dwordx4 %0, %1, %2" : "=v"(r) : "v"(off), "s"(base) : "memory"); }
__device__ __forceinline__ void st8_wt(const void* base, uint32_t off, u32x2 v) {
    asm volatile("global_store_dwordx2 %0, %1, %2 sc1" :: "v"(off), "v"(v), "s"(base) : "memory");
}
__device__ __forceinline__ void poll_flags(u32x2 f, const void* base, uint32_t off, uint32_t t0, uint32_t t1) {
#pragma unroll 1
    for (int spin = 0; spin < (1 << 16); ++spin) {
        if (__builtin_amdgcn_ballot_w64(f[0] != t0 || f[1] != t1) == 0ull) break;
        __builtin_amdgcn_s_sleep(POLL_SLEEP);
        asm volatile("global_load_dwordx2 %0, %1, %2 sc1\n\ts_waitcnt vmcnt(0)" : "=&v"(f) : "v"(off), "s"(base) : "memory");
    }
    asm volatile("" ::: "memory");
}
__device__ __forceinline__ void poll_flags2(u32x4 f, const void* base, uint32_t off, uint32_t t0, uint32_t t1) {
#pragma unroll 1
    for (int spin = 0; spin < (1 << 16); ++spin) {
        if (__builtin_amdgcn_ballot_w64(f[0] != t0 || f[1] != t1 || f[2] != t0 || f[3] != t1) == 0ull) break;
        __builtin_amdgcn_s_sleep(POLL_SLEEP);
        asm volatile("global_load_dwordx4 %0, %1, %2 sc1\n\ts_waitcnt vmcnt(0)" : "=&v"(f) : "v"(off), "s"(base) : "memory");
    }
    asm volatile("" ::: "memory");
}
__device__ __forceinline__ void ld16_dev_w123(int wave, u32x4& r, const void* base, uint32_t off) {
    asm volatile("s_cmp_gt_u32 %3, 2\n\ts_cbranch_scc1 1f\n\tglobal_load_dwordx4 %0, %1, %2 sc1\n1:" : "=v"(r) : "v"(off), "s"(base), "s"(wave - 1) : "memory", "scc");
}
__device__ __forceinline__ void ld8_dev_w0(int wave, u32x2& r, const void* base, uint32_t off) {
    asm volatile("s_cmp_lg_u32 %3, 0\n\ts_cbranch_scc1 1f\n\tglobal_load_dwordx2 %0, %1, %2 sc1\n1:" : "=v"(r) : "v"(off), "s"(base), "s"(wave) : "memory", "scc");
}
#define WAIT2(c, a, b) do { if (c) asm volatile("s_waitcnt vmcnt(%0) lgkmcnt(0)" :: "i"(a) : "memory"); else asm volatile("s_waitcnt vmcnt(%0) lgkmcnt(0)" :: "i"(b) : "memory"); } while (0)
__device__ __forceinline__ f32x2 cmul(f32x2 a, f32x2 b) { return a.xx * b + a.yy * (f32x2){-b.y, b.x}; }
__device__ __forceinline__ f32x2 cmulc(f32x2 a, f32x2 b) { return a.xx * (f32x2){b.x, -b.y} + a.yy * b.yx; }
#define ROT_A(P, M) P " %0, %8, %11\n\t" M " %1, %9, %10\n\t" M " %2, %8, %11\n\t" P " %3, %9, %10\n\t" \
                    P " %4, %12, %13\n\t" M " %5, %13, %12\n\t" M " %6, %14, %15\n\t" P " %7, %15, %14"
#define ROT_B(P, M, NP, NM) P " %0, %8, %11\n\t" M " %1, %9, %10\n\t" M " %2, %8, %11\n\t" P " %3, %9, %10\n\t" \
                    "v_fma_f32 %4, " NP "%15, %16, %12\n\tv_fma_f32 %5, " NM "%14, %16, %13\n\tv_fma_f32 %6, " NM "%15, %16, %12\n\tv_fma_f32 %7, " NP "%14, %16, %13"
template <int SG> __device__ __forceinline__ void dft8(f32x2 (&x)[8]) {
    const float h = 0.70710678118654752440f;
    const f32x2 t0 = x[0] + x[4], t4 = x[0] - x[4], t2 = x[2] + x[6], t6 = x[2] - x[6];
    const f32x2 t1 = x[1] + x[5], t5 = x[1] - x[5], t3 = x[3] + x[7], t7 = x[3] - x[7];
    float o0, o1, o2, o3, o4, o5, o6, o7;
    if (SG > 0) asm(ROT_A("v_sub_f32", "v_add_f32") : "=&v"(o0), "=&v"(o1), "=&v"(o2), "=&v"(o3), "=&v"(o4), "=&v"(o5), "=&v"(o6), "=&v"(o7)
                    : "v"(t4.x), "v"(t4.y), "v"(t6.x), "v"(t6.y), "v"(t5.x), "v"(t5.y), "v"(t7.x), "v"(t7.y));
    else asm(ROT_A("v_add_f32", "v_sub_f32") : "=&v"(o0), "=&v"(o1), "=&v"(o2), "=&v"(o3), "=&v"(o4), "=&v"(o5), "=&v"(o6), "=&v"(o7)
             : "v"(t4.x), "v"(t4.y), "v"(t6.x), "v"(t6.y), "v"(t5.x), "v"(t5.y), "v"(t7.x), "v"(t7.y));
    const f32x2 a = {o0, o1}, u5 = {o4, o5}, n7 = {o6, o7};
    const f32x2 s0 = t0 + t2, s1 = t0 - t2, s2 = t1 + t3, s3 = t1 - t3;
    const f32x2 p = u5 - n7, q = u5 + n7;
    x[0] = s0 + s2; x[4] = s0 - s2;
    x[1] = a + p * h; x[5] = a - p * h;
    float r0, r1, r2, r3, r4, r5, r6, r7;
    if (SG > 0) asm(ROT_B("v_sub_f32", "v_add_f32", "-", "") : "=&v"(r0), "=&v"(r1), "=&v"(r2), "=&v"(r3), "=&v"(r4), "=&v"(r5), "=&v"(r6), "=&v"(r7)
                    : "v"(s1.x), "v"(s1.y), "v"(s3.x), "v"(s3.y), "v"(o2), "v"(o3), "v"(q.x), "v"(q.y), "s"(h));
    else asm(ROT_B("v_add_f32", "v_sub_f32", "", "-") : "=&v"(r0), "=&v"(r1), "=&v"(r2), "=&v"(r3), "=&v"(r4), "=&v"(r5), "=&v"(r6), "=&v"(r7)
             : "v"(s1.x), "v"(s1.y), "v"(s3.x), "v"(s3.y), "v"(o2), "v"(o3), "v"(q.x), "v"(q.y), "s"(h));
    x[2] = (f32x2){r0, r1}; x[6] = (f32x2){r2, r3}; x[3] = (f32x2){r4, r5}; x[7] = (f32x2){r6, r7};
}
#define CM1(o, a, b, N) "v_pk_mul_f32 %" #o ", %" #a ", %" #b " op_sel:[0,0] op_sel_hi:[0,1]" N "\n\t"
#define CM2(o, a, b, N) "v_pk_fma_f32 %" #o ", %" #a ", %" #b ", %" #o " op_sel:[1,1,0] op_sel_hi:[1,0,1]" N "\n\t"
#define CMUL4_BODY(N1, N2) CM1(0, 4, 8, N1) CM1(1, 5, 9, N1) CM1(2, 6, 10, N1) CM1(3, 7, 11, N1) CM2(0, 4, 8, N2) CM2(1, 5, 9, N2) CM2(2, 6, 10, N2) CM2(3, 7, 11, N2)
#define CMUL3_BODY(N1, N2) CM1(0, 3, 6, N1) CM1(1, 4, 7, N1) CM1(2, 5, 8, N1) CM2(0, 3, 6, N2) CM2(1, 4, 7, N2) CM2(2, 5, 8, N2)
template <bool CONJ> __device__ __forceinline__ void cmul4(f32x2& a0, f32x2& a1, f32x2& a2, f32x2& a3, f32x2 b0, f32x2 b1, f32x2 b2, f32x2 b3) {
    f32x2 o0, o1, o2, o3;
    if (CONJ) asm(CMUL4_BODY(" neg_hi:[1,0]", "") : "=&v"(o0), "=&v"(o1), "=&v"(o2), "=&v"(o3) : "v"(a0), "v"(a1), "v"(a2), "v"(a3), "v"(b0), "v"(b1), "v"(b2), "v"(b3));
    else asm(CMUL4_BODY("", " neg_lo:[1,0,0]") : "=&v"(o0), "=&v"(o1), "=&v"(o2), "=&v"(o3) : "v"(a0), "v"(a1), "v"(a2), "v"(a3), "v"(b0), "v"(b1), "v"(b2), "v"(b3));
    a0 = o0; a1 = o1; a2 = o2; a3 = o3;
}
template <bool CONJ> __device__ __forceinline__ void cmul3(f32x2& a0, f32x2& a1, f32x2& a2, f32x2 b0, f32x2 b1, f32x2 b2) {
    f32x2 o0, o1, o2;
    if (CONJ) asm(CMUL3_BODY(" neg_hi:[1,0]", "") : "=&v"(o0), "=&v"(o1), "=&v"(o2) : "v"(a0), "v"(a1), "v"(a2), "v"(b0), "v"(b1), "v"(b2));
    else asm(CMUL3_BODY("", " neg_lo:[1,0,0]") : "=&v"(o0), "=&v"(o1), "=&v"(o2) : "v"(a0), "v"(a1), "v"(a2), "v"(b0), "v"(b1), "v"(b2));
    a0 = o0; a1 = o1; a2 = o2;
}
template <bool CONJ> __device__ __forceinline__ void twiddle7(f32x2 (&x)[8], const f32x2 (&tw)[7]) {
    cmul3<CONJ>(x[1], x[2], x[3], tw[0], tw[1], tw[2]);
    cmul4<CONJ>(x[4], x[5], x[6], x[7], tw[3], tw[4], tw[5], tw[6]);
}
#define QP2 " quad_perm:[2,3,0,1] row_mask:0xf bank_mask:0xf\n\t"
#define QP1 " quad_perm:[1,0,3,2] row_mask:0xf bank_mask:0xf\n\t"
#define QS1 "v_fmac_f32_dpp %0, %0, %16" QP2 "v_fmac_f32_dpp %1, %1, %16" QP2 "v_fmac_f32_dpp %2, %2, %16" QP2 "v_fmac_f32_dpp %3, %3, %16" QP2 \
            "v_fmac_f32_dpp %4, %4, %16" QP2 "v_fmac_f32_dpp %5, %5, %16" QP2 "v_fmac_f32_dpp %6, %6, %16" QP2 "v_fmac_f32_dpp %7, %7, %16" QP2
#define QS2 "v_cndmask_b32 %8, %0, %1, %18\n\t" "v_cndmask_b32 %9, %1, -%0, %18\n\t" "v_cndmask_b32 %10, %2, %3, %18\n\t" "v_cndmask_b32 %11, %3, -%2, %18\n\t" \
            "v_cndmask_b32 %12, %4, %5, %18\n\t" "v_cndmask_b32 %13, %5, -%4, %18\n\t" "v_cndmask_b32 %14, %6, %7, %18\n\t" "v_cndmask_b32 %15, %7, -%6, %18\n\t" \
            "v_fmac_f32_dpp %0, %8, %17" QP1 "v_fmac_f32_dpp %1, %9, %17" QP1 "v_fmac_f32_dpp %2, %10, %17" QP1 "v_fmac_f32_dpp %3, %11, %17" QP1 \
            "v_fmac_f32_dpp %4, %12, %17" QP1 "v_fmac_f32_dpp %5, %13, %17" QP1 "v_fmac_f32_dpp %6, %14, %17" QP1 "v_fmac_f32_dpp %7, %15, %17" QP1
template <bool INV> __device__ __forceinline__ void quad4(f32x2& e0, f32x2& e1, f32x2& e2, f32x2& e3, float sg, float cc) {
    float a0 = e0.x, a1 = e0.y, a2 = e1.x, a3 = e1.y, a4 = e2.x, a5 = e2.y, a6 = e3.x, a7 = e3.y, q0, q1, q2, q3, q4, q5, q6, q7;
    const uint64_t hi = 0xccccccccccccccccull;
    if (INV)
        asm("s_nop 1\n\t" QS2 QS1 : "+v"(a0), "+v"(a1), "+v"(a2), "+v"(a3), "+v"(a4), "+v"(a5), "+v"(a6), "+v"(a7),
            "=&v"(q0), "=&v"(q1), "=&v"(q2), "=&v"(q3), "=&v"(q4), "=&v"(q5), "=&v"(q6), "=&v"(q7) : "v"(sg), "v"(cc), "s"(hi));
    else
        asm("s_nop 1\n\t" QS1 QS2 : "+v"(a0), "+v"(a1), "+v"(a2), "+v"(a3), "+v"(a4), "+v"(a5), "+v"(a6), "+v"(a7),
            "=&v"(q0), "=&v"(q1), "=&v"(q2), "=&v"(q3), "=&v"(q4), "=&v"(q5), "=&v"(q6), "=&v"(q7) : "v"(sg), "v"(cc), "s"(hi));
    e0 = (f32x2){a0, a1}; e1 = (f32x2){a2, a3}; e2 = (f32x2){a4, a5}; e3 = (f32x2){a6, a7};
}
__device__ __forceinline__ f32x2 twid(int n, float inv) {
    const float a = (float)n * inv;
    return (f32x2){__builtin_amdgcn_cosf(a), -__builtin_amdgcn_sinf(a)};
}
struct cf { float x, y; };
#define BF4 "v_add_f32 %0, %8, %12\n\tv_add_f32 %1, %9, %13\n\tv_add_f32 %2, %10, %14\n\tv_add_f32 %3, %11, %15\n\t" \
            "v_sub_f32 %4, %8, %12\n\tv_sub_f32 %5, %9, %13\n\tv_sub_f32 %6, %10, %14\n\tv_sub_f32 %7, %11, %15"
__device__ __forceinline__ void bf4(float& s0, float& s1, float& s2, float& s3, float& d0, float& d1, float& d2, float& d3,
                                    float a0, float a1, float a2, float a3, float b0, float b1, float b2, float b3) {
    asm(BF4 : "=&v"(s0), "=&v"(s1), "=&v"(s2), "=&v"(s3), "=&v"(d0), "=&v"(d1), "=&v"(d2), "=&v"(d3)
        : "v"(a0), "v"(a1), "v"(a2), "v"(a3), "v"(b0), "v"(b1), "v"(b2), "v"(b3));
}
#define BFH4 "v_fma_f32 %0, %12, %16, %8\n\tv_fma_f32 %1, %13, %16, %9\n\tv_fma_f32 %2, %14, %16, %10\n\tv_fma_f32 %3, %15, %16, %11\n\t" \
             "v_fma_f32 %4, -%12, %16, %8\n\tv_fma_f32 %5, -%13, %16, %9\n\tv_fma_f32 %6, -%14, %16, %10\n\tv_fma_f32 %7, -%15, %16, %11"
__device__ __forceinline__ void bfh4(float& s0, float& s1, float& s2, float& s3, float& d0, float& d1, float& d2, float& d3,
                                     float a0, float a1, float a2, float a3, float b0, float b1, float b2, float b3) {
    asm(BFH4 : "=&v"(s0), "=&v"(s1), "=&v"(s2), "=&v"(s3), "=&v"(d0), "=&v"(d1), "=&v"(d2), "=&v"(d3)
        : "v"(a0), "v"(a1), "v"(a2), "v"(a3), "v"(b0), "v"(b1), "v"(b2), "v"(b3), "s"(0.70710678118654752440f));
}
template <int SG> __device__ __forceinline__ void dft8s(f32x2 (&x)[8]) {
    cf t0, t1, t2, t3, t4, t5, t6, t7;
    bf4(t0.x, t0.y, t2.x, t2.y, t4.x, t4.y, t6.x, t6.y, x[0].x, x[0].y, x[2].x, x[2].y, x[4].x, x[4].y, x[6].x, x[6].y);
    bf4(t1.x, t1.y, t3.x, t3.y, t5.x, t5.y, t7.x, t7.y, x[1].x, x[1].y, x[3].x, x[3].y, x[5].x, x[5].y, x[7].x, x[7].y);
    cf s0, s1, s2, s3, a, b, c, d;
    bf4(s0.x, s0.y, s2.x, s2.y, s1.x, s1.y, s3.x, s3.y, t0.x, t0.y, t1.x, t1.y, t2.x, t2.y, t3.x, t3.y);
    bf4(a.x, b.y, c.x, d.y, b.x, a.y, d.x, c.y, t4.x, t4.y, t5.x, t5.y, t6.y, t6.x, t7.y, t7.x);
    cf X0, X4, X2, X6, X1, X5, X3, X7;
    bf4(X0.x, X0.y, X2.x, X6.y, X4.x, X4.y, X6.x, X2.y, s0.x, s0.y, s1.x, s1.y, s2.x, s2.y, s3.y, s3.x);
    float px, py, qx, qy;
    asm("v_add_f32 %0, %4, %5\n\tv_sub_f32 %1, %5, %4\n\tv_sub_f32 %2, %6, %7\n\tv_add_f32 %3, %6, %7" : "=&v"(px), "=&v"(py), "=&v"(qx), "=&v"(qy) : "v"(c.x), "v"(c.y), "v"(d.x), "v"(d.y));
    bfh4(X1.x, X1.y, X7.x, X7.y, X5.x, X5.y, X3.x, X3.y, a.x, a.y, b.x, b.y, px, py, qx, qy);
    x[0] = (f32x2){X0.x, X0.y}; x[4] = (f32x2){X4.x, X4.y};
    if (SG < 0) {
        x[1] = (f32x2){X1.x, X1.y}; x[2] = (f32x2){X2.x, X2.y}; x[3] = (f32x2){X3.x, X3.y}; x[5] = (f32x2){X5.x, X5.y}; x[6] = (f32x2){X6.x, X6.y}; x[7] = (f32x2){X7.x, X7.y};
    } else {
        x[7] = (f32x2){X1.x, X1.y}; x[6] = (f32x2){X2.x, X2.y}; x[5] = (f32x2){X3.x, X3.y}; x[3] = (f32x2){X5.x, X5.y}; x[2] = (f32x2){X6.x, X6.y}; x[1] = (f32x2){X7.x, X7.y};
    }
}
#define CMUL2(N1, N2) "v_mul_f32 %0, %4, %8\n\tv_mul_f32 %1, %5, %8\n\tv_mul_f32 %2, %6, %10\n\tv_mul_f32 %3, %7, %10\n\t" \
                      "v_fma_f32 %0, " N1 "%5, %9, %0\n\tv_fma_f32 %1, " N2 "%4, %9, %1\n\tv_fma_f32 %2, " N1 "%7, %11, %2\n\tv_fma_f32 %3, " N2 "%6, %11, %3"
template <bool CONJ> __device__ __forceinline__ void cmul2(f32x2& a0, f32x2& a1, f32x2 b0, f32x2 b1) {
    float o0, o1, o2, o3;
    if (CONJ) asm(CMUL2("", "-") : "=&v"(o0), "=&v"(o1), "=&v"(o2), "=&v"(o3) : "v"(a0.x), "v"(a0.y), "v"(a1.x), "v"(a1.y), "v"(b0.x), "v"(b0.y), "v"(b1.x), "v"(b1.y));
    else asm(CMUL2("-", "") : "=&v"(o0), "=&v"(o1), "=&v"(o2), "=&v"(o3) : "v"(a0.x), "v"(a0.y), "v"(a1.x), "v"(a1.y), "v"(b0.x), "v"(b0.y), "v"(b1.x), "v"(b1.y));
    a0 = (f32x2){o0, o1}; a1 = (f32x2){o2, o3};
}
template <bool CONJ> __device__ __forceinline__ void cmul1(f32x2& a0, f32x2 b0) {
    float o0, o1;
    if (CONJ) asm("v_mul_f32 %0, %2, %4\n\tv_mul_f32 %1, %3, %4\n\tv_fma_f32 %0, %3, %5, %0\n\tv_fma_f32 %1, -%2, %5, %1" : "=&v"(o0), "=&v"(o1) : "v"(a0.x), "v"(a0.y), "v"(b0.x), "v"(b0.y));
    else asm("v_mul_f32 %0, %2, %4\n\tv_mul_f32 %1, %3, %4\n\tv_fma_f32 %0, -%3, %5, %0\n\tv_fma_f32 %1, %2, %5, %1" : "=&v"(o0), "=&v"(o1) : "v"(a0.x), "v"(a0.y), "v"(b0.x), "v"(b0.y));
    a0 = (f32x2){o0, o1};
}
template <bool CONJ> __device__ __forceinline__ void cmul4s(f32x2& a0, f32x2& a1, f32x2& a2, f32x2& a3, f32x2 b0, f32x2 b1, f32x2 b2, f32x2 b3) {
    cmul2<CONJ>(a0, a1, b0, b1); cmul2<CONJ>(a2, a3, b2, b3);
}
template <bool CONJ> __device__ __forceinline__ void twiddle7s(f32x2 (&x)[8], const f32x2 (&tw)[7]) {
    cmul2<CONJ>(x[1], x[2], tw[0], tw[1]); cmul2<CONJ>(x[3], x[4], tw[2], tw[3]); cmul2<CONJ>(x[5], x[6], tw[4], tw[5]); cmul1<CONJ>(x[7], tw[6]);
}
#define EARLY2 0
#define ROTK 0
#define REL_AT 0
#define ZDQ 1
#define ABL_HOT 0
#define TAB_SPLIT 4
#define FS(i) do {} while (0)
#define ABL_S1 0
#define ABL_S2 0
#define ABL_W 0
constexpr int LDS_BYTES = 163840;
constexpr int A_XRING = 98304, L_SPARE = 147456;
constexpr int X_FR = 0, X_FI = 33792, X_TF = 65536, X_CS = 122880, L_SINK = L_SPARE;
#define POLL_IT 6
constexpr int L_SYNC = A_XRING, L_FBUF = A_XRING + 64;

__device__ __forceinline__ float row_var_exact(const uint16_t* xpart, float mu) {
    float s = 0.f;
#pragma unroll 1
    for (int c = 0; c < 8; ++c) {
        const u32x4 v = *(const u32x4*)(xpart + c * 8);
#pragma unroll
        for (int e = 0; e < 4; ++e) { const uint32_t u = v[e]; const float a = bf_lo(u) - mu, b = bf_hi(u) - mu; s = __builtin_fmaf(a, a, s); s = __builtin_fmaf(b, b, s); }
    }
    s += dppf<0xB1>(s); s += dppf<0x4E>(s);
    s += __shfl_xor(s, 32);
    return s * (1.0f / 512.0f);
}
__device__ __forceinline__ float smul(float a, float b) { float r; asm("v_mul_f32 %0, %1, %2" : "=v"(r) : "v"(a), "v"(b)); return r; }
__device__ __forceinline__ float sfms(float a, float b, float c) { float r; asm("s_nop 0\n\tv_fma_f32 %0, %1, %2, -%3" : "=v"(r) : "v"(a), "v"(b), "v"(c)); return r; }
__device__ __forceinline__ void poll_now(const void* base, uint32_t off, uint32_t t0, uint32_t t1) {
    u32x2 f;
    asm volatile("global_load_dwordx2 %0, %1, %2 sc1\n\ts_waitcnt vmcnt(0)" : "=&v"(f) : "v"(off), "s"(base) : "memory");
    poll_flags(f, base, off, t0, t1);
}
template <int SLEEP = 0> __device__ __forceinline__ void lds_wait_ge(const char* lds, int off, uint32_t target) {
#pragma unroll 1
    for (int spin = 0; spin < (1 << 22); ++spin) {
        const uint32_t c = __builtin_amdgcn_readfirstlane(*(volatile LDSP uint32_t*)(lds + off));
        if (c >= target) break;
        if (SLEEP) __builtin_amdgcn_s_sleep(SLEEP);
    }
    asm volatile("" ::: "memory");
}

__global__ __launch_bounds__(512, 2) void fused_kernel(const uint16_t* __restrict__ x, const uint16_t* __restrict__ fr, const uint16_t* __restrict__ fi,
                                                       const uint16_t* __restrict__ wg, const uint16_t* __restrict__ wu, const uint16_t* __restrict__ wd,
                                                       char* __restrict__ ws, uint16_t* __restrict__ out) {
    __shared__ __attribute__((aligned(1024))) char lds[LDS_BYTES];
    {
        uint64_t pc0;
        uint32_t keep, e = ((blockIdx.x >> 3) & (CODE_SPLIT - 1)) + CODE_SPLIT * (((threadIdx.x >> 3) & 56) | (threadIdx.x & 7));
        e = e < CODE_LINES ? e : CODE_LINES - 1;
        asm volatile("s_getpc_b64 %0\n\ts_mov_b32 %1, m0\n\ts_mov_b32 m0, %3\n\ts_mov_b64 exec, 0xff\n\tglobal_load_lds_dword %2, %0\n\ts_mov_b64 exec, -1\n\ts_mov_b32 m0, %1"
                     : "=&s"(pc0), "=&s"(keep) : "v"(e * 128), "s"((uint32_t)(uintptr_t)(LDSP char*)lds + L_SINK) : "memory");
        __builtin_amdgcn_sched_barrier(0);
    }
    TS(0);
    const int w = __builtin_amdgcn_readfirstlane(threadIdx.x >> 6);
    const int b = blockIdx.x, g = b & 7, idx = b >> 3;
    const uint32_t ldsb = (uint32_t)(uintptr_t)(LDSP char*)lds;
    const int gi = idx - 4;
    const int mt = 4 * (g & 3) + (gi & 3), nt = 7 * (g >> 2) + (gi >> 2);
    const int l0 = threadIdx.x & 63;
    const int cp = w & 3;
    const char* wbase = (const char*)((cp >> 1) ? wu : wg) + 256 * nt + 128 * (cp & 1) + (size_t)(32 * (w >> 2)) * 3584;
    const uint32_t wo0 = (uint32_t)(((l0 >> 2) & 7) * 3584 + 64 * (l0 >> 5) + 16 * (l0 & 3));
    const uint32_t wo1 = wo0 + 8 * 3584, wo2 = wo0 + 16 * 3584, wo3 = wo0 + 24 * 3584;
    const uint32_t wdst = ldsb + 2 * (w >> 2) * 24576 + cp * 1024;
    const char* xbase = (const char*)x + (size_t)mt * 131072;
    uint32_t xo0, xo1;
    {
        const int R0 = 16 * w + (l0 >> 3), R1 = R0 + 8;
        xo0 = (uint32_t)((((R0 & ~63) | ((R0 & 31) << 1) | ((R0 >> 5) & 1)) << 10) + (((l0 & 7) ^ ((R0 >> 1) & 7)) << 4));
        xo1 = (uint32_t)((((R1 & ~63) | ((R1 & 31) << 1) | ((R1 >> 5) & 1)) << 10) + (((l0 & 7) ^ ((R1 >> 1) & 7)) << 4));
    }
    const uint32_t xdst = ldsb + A_XRING + w * 2048;
    auto stage = [&](int it) {
        const int ks_ = (it + (ROTK == 1 ? 2 * (g & 3) + (g >> 2) : ROTK == 2 ? 4 * (g & 1) : 0)) & 7;
        const char* wsrc = wbase + (size_t)(64 * ks_) * 3584;
        const char* xsrc = xbase + ks_ * 128;
        uint32_t keep;
        asm volatile("s_cmp_lt_i32 %11, 0\n\ts_cbranch_scc1 1f\n\ts_mov_b32 %0, m0\n\t"
                     "s_mov_b32 m0, %1\n\ts_nop 0\n\tglobal_load_lds_dwordx4 %3, %9\n\t"
                     "s_add_u32 m0, m0, 0x400\n\ts_nop 0\n\tglobal_load_lds_dwordx4 %4, %9\n\t"
                     "s_mov_b32 m0, %2\n\ts_nop 0\n\tglobal_load_lds_dwordx4 %5, %10\n\t"
                     "s_add_u32 m0, m0, 0x1000\n\ts_nop 0\n\tglobal_load_lds_dwordx4 %6, %10\n\t"
                     "s_add_u32 m0, m0, 0x5000\n\ts_nop 0\n\tglobal_load_lds_dwordx4 %7, %10\n\t"
                     "s_add_u32 m0, m0, 0x1000\n\ts_nop 0\n\tglobal_load_lds_dwordx4 %8, %10\n\t"
                     "s_mov_b32 m0, %0\n1:"
                     : "=&s"(keep) : "s"(xdst + (it % 3) * 16384), "s"(wdst + (it % 3) * 8192), "v"(xo0), "v"(xo1), "v"(wo0), "v"(wo1), "v"(wo2), "v"(wo3), "s"(xsrc), "s"(wsrc), "s"(it == 0 ? gi : 0)
                     : "memory", "scc");
    };
    auto piece = [&](int it, int p) {
        const int ks_ = it;
        const char* src = p < 2 ? xbase + ks_ * 128 : wbase + (size_t)(64 * ks_) * 3584;
        const uint32_t dst = p < 2 ? xdst + (it % 3) * 16384 + p * 1024 : wdst + (it % 3) * 8192 + ((p - 2) & 1) * 4096 + ((p - 2) >> 1) * 24576;
        glds1(dst, p == 0 ? xo0 : p == 1 ? xo1 : p == 2 ? wo0 : p == 3 ? wo1 : p == 4 ? wo2 : wo3, src);
    };
    stage(0);
    {
        const char* src = (const char*)x + (size_t)(64 * (8 * (g & 3) + 4 * (g >> 2) + idx) + 8 * w) * 1024;
        uint32_t keep, v0 = (uint32_t)(l0 << 4), v1;
#define XR1(n) "v_xor_b32 %1, " #n ", %2\n\ts_add_u32 m0, m0, 0x400\n\ts_nop 0\n\tglobal_load_lds_dwordx4 %1, %4\n\t"
        asm volatile("s_cmp_ge_i32 %5, 0\n\ts_cbranch_scc1 1f\n\ts_mov_b32 %0, m0\n\ts_mov_b32 m0, %3\n\ts_nop 0\n\tglobal_load_lds_dwordx4 %2, %4\n\t"
                     XR1(0x420) XR1(0x840) XR1(0xc60) XR1(0x1080) XR1(0x14a0) XR1(0x18c0) XR1(0x1ce0) "s_mov_b32 m0, %0\n1:"
                     : "=&s"(keep), "=&v"(v1) : "v"(v0), "s"(ldsb + w * 8192), "s"(src), "s"(gi) : "memory", "scc");
    }
    __builtin_amdgcn_sched_barrier(0);
    const bool aux = idx < 4;
    const bool fftw = w < 4;
    const int kp = g * 8 + (idx & 7), var = idx >> 3;
    const int kpe = (kp == 0 ? 64 : kp) & -(int)((kp != 0) | (var & 1));
    const int k = 128 * ((var + 1) >> 1) + ((var & 1) ? -kpe : kpe);
    const int sp = 8 * (g & 3) + 4 * (g >> 2) + (idx & 3), dp = idx >> 2;

    u32x2 tagv;
    uint32_t tg0, tg1;
    int tid, l, hh, l31, t, fp, fu, fq, fv, fm;
    uint32_t wdo, wdrd, wtro, flo, lo;
    auto refresh = [&]() {
        tid = threadIdx.x;
        asm volatile("" : "+v"(tid));
        l = tid & 63; hh = l >> 5; l31 = l & 31; t = tid & 255;
        fp = t >> 5; fu = t & 31; fq = (t >> 2) & 7; fv = t & 3; fm = ((fv & 1) << 1) | (fv >> 1);
        wdo = (uint32_t)(((l >> 2) & 7) * 1024 + 64 * hh + 16 * (l & 3));
        wdrd = (uint32_t)((4 * hh + ((l >> 2) & 3)) * 64 + ((l >> 4) & 1) * 32 + (l & 3) * 8);
        wtro = (uint32_t)(((l >> 3) + 8 * (l & 7)) * 514);
        flo = hh ? (uint32_t)(((k >> 3) * 32 + l31) * 8) : (uint32_t)(WS_FA - WS_FW + 512 + l31 * 8);
        lo = l * 16;
    };
    refresh();

    const int wv = w & 3;
    auto wdplace = [&](int i) -> uint32_t {
        const int j = i & 15;
        return j < 2 ? (uint32_t)(L_SPARE + wv * 4096 + j * 2048) : j < 6 ? (uint32_t)(wv * 24576 + 16384 + (j - 2) * 2048)
             : j < 8 ? (uint32_t)(A_XRING + 32768 + wv * 4096 + (j - 6) * 2048) : (uint32_t)(wv * 24576 + (j - 8) * 2048);
    };
    auto wdstage = [&](int i) {
        uint32_t keep;
        asm volatile("s_mov_b32 %0, m0\n\t"
                     "s_mov_b32 m0, %1\n\ts_nop 0\n\tglobal_load_lds_dwordx4 %2, %4\n\t"
                     "s_add_u32 m0, m0, 0x400\n\ts_nop 0\n\tglobal_load_lds_dwordx4 %3, %4\n\t"
                     "s_mov_b32 m0, %0"
                     : "=&s"(keep) : "s"(ldsb + wdplace(i)), "v"(wdo), "v"(wdo + 8192), "s"((const char*)wd + (size_t)(28 * wv + i) * 16384 + dp * 128) : "memory", "scc");
    };
    auto wdstages = [&](int i0, int i1) {
        if (!fftw) {
#pragma unroll
            for (int i = i0; i < i1; ++i) wdstage(i);
        }
    };
    auto wdread = [&](int i, int dt) -> u32x4 {
        const char* p = lds + wdplace(i) + wdrd + dt * 512;
        bf16x4 p0 = __builtin_amdgcn_ds_read_tr16_b64_v4bf16((LDSP bf16x4*)p);
        bf16x4 p1 = __builtin_amdgcn_ds_read_tr16_b64_v4bf16((LDSP bf16x4*)(p + 1024));
        return __builtin_bit_cast(u32x4, __builtin_shufflevector(p0, p1, 0, 1, 2, 3, 4, 5, 6, 7));
    };
    u32x4 ein[8];
    uint32_t wt[8], w3[8];
    f32x2 uq[8], wv2[8];
    f32x2 twA[7], twB[7], twC[7];
    u32x4 ev[8];
    uint32_t cr[4], ci[4];
    auto fft_twiddles = [&]() {
#pragma unroll
        for (int i = 0; i < 7; ++i) {
            const f32x2 a = twid(t * (i + 1), 1.0f / 2048.0f), c = twid(fu * (i + 1), 1.0f / 256.0f);
            ev[i] = __builtin_bit_cast(u32x4, (f32x4){a.x, a.y, c.x, c.y});
            asm volatile("" : "+v"(ev[i]));
        }
    };
    auto fft_consts = [&]() {
#pragma unroll
        for (int i = 0; i < 7; ++i) { twC[i] = twid(fv * (i + 1), 1.0f / 32.0f); asm volatile("" : "+v"(twC[i])); }
#pragma unroll
        for (int r = 0; r < 4; ++r) {
            const f32x2 wv = twid(r * k, 1.0f / 512.0f);
            f32x2 al, be;
            if (kp == 0) {
                al = k == 0 ? (f32x2){1.f, (r & 1) ? -1.f : 1.f} : (var & 1) ? (f32x2){0.f, 0.f} : wv;
                be = (var & 1) ? wv : (f32x2){0.f, 0.f};
            } else {
                al = wv;
                be = (var & 1) ? (f32x2){wv.y, -wv.x} : (f32x2){-wv.y, wv.x};
            }
            cr[r] = __builtin_amdgcn_readfirstlane(pk_f16(al.x, be.x)); ci[r] = __builtin_amdgcn_readfirstlane(pk_f16(al.y, be.y));
        }
    };
    auto e_loads = [&](int a0, int sel, int second) {
        const char* p = ws + WS_E + (size_t)kp * 32768 + a0 * 4096;
#define ELD "s_cmp_eq_u32 %9, 0\n\ts_cbranch_scc1 1f\n\tglobal_load_dwordx4 %0, %4, %5 sc1\n\tglobal_load_dwordx4 %1, %4, %6 sc1\n\tglobal_load_dwordx4 %2, %4, %7 sc1\n\tglobal_load_dwordx4 %3, %4, %8 sc1\n1:"
        if (second) asm volatile(ELD : "+v"(ein[a0]), "+v"(ein[a0 + 1]), "+v"(ein[a0 + 2]), "+v"(ein[a0 + 3]) : "v"((uint32_t)(t * 16)), "s"(p), "s"(p + 4096), "s"(p + 8192), "s"(p + 12288), "s"(sel) : "memory", "scc");
        else asm volatile(ELD : "=&v"(ein[a0]), "=&v"(ein[a0 + 1]), "=&v"(ein[a0 + 2]), "=&v"(ein[a0 + 3]) : "v"((uint32_t)(t * 16)), "s"(p), "s"(p + 4096), "s"(p + 8192), "s"(p + 12288), "s"(sel) : "memory", "scc");
    };
    auto w_loads = [&](int sel, int second) {
        const uint32_t wo = (uint32_t)(fm * 2048 + (fp * 8 + fq) * 4);
#define WL1(r, o) "global_load_dword %" #r ", %8, %9 offset:" #o " sc1\n\t"
#define WLD "s_cmp_eq_u32 %10, 0\n\ts_cbranch_scc1 1f\n\t" WL1(0, 0) WL1(1, 256) WL1(2, 512) WL1(3, 768) WL1(4, 1024) WL1(5, 1280) WL1(6, 1536) WL1(7, 1792) "\n1:"
        if (second) asm volatile(WLD : "+v"(wt[0]), "+v"(wt[1]), "+v"(wt[2]), "+v"(wt[3]), "+v"(wt[4]), "+v"(wt[5]), "+v"(wt[6]), "+v"(wt[7]) : "v"(wo), "s"(ws + WS_WT + (size_t)k * 8192), "s"(sel) : "memory", "scc");
        else asm volatile(WLD : "=&v"(wt[0]), "=&v"(wt[1]), "=&v"(wt[2]), "=&v"(wt[3]), "=&v"(wt[4]), "=&v"(wt[5]), "=&v"(wt[6]), "=&v"(wt[7]) : "v"(wo), "s"(ws + WS_WT + (size_t)k * 8192), "s"(sel) : "memory", "scc");
    };
    const int fsel = (w >> 2) ^ 1;
    int early = 0;
    auto tie_fft = [&](bool special) {
#pragma unroll
        for (int a = 0; a < 8; ++a) {
            asm volatile("" : "+v"(ein[a]), "+v"(wt[a]));
        }
    };
    auto touch = [&](uint32_t voff, const void* base) {
        uint32_t keep;
        asm volatile("s_mov_b32 %0, m0\n\ts_mov_b32 m0, %1\n\ts_mov_b64 exec, 0xff\n\tglobal_load_lds_dword %2, %3\n\ts_mov_b64 exec, -1\n\ts_mov_b32 m0, %0" : "=&s"(keep) : "s"(ldsb + L_SINK), "v"(voff), "s"(base) : "memory");
    };
    auto tag_set = [&]() {
        asm volatile("" : "+v"(tagv));
        tg0 = __builtin_amdgcn_readfirstlane(tagv[0]) ^ 0x9e3779b9u; tg1 = __builtin_amdgcn_readfirstlane(tagv[1]) ^ 0x7f4a7c15u;
    };
    u32x2 wfl;
    const int fa = b & 31, fkb = ((b >> 5) + 3) & 7;
    auto wt_stage = [&]() {
        const uint32_t lim = 2048u * DF_ * 2 - 16;
        const int c = (l & 7) ^ (l >> 3) ^ 6;
        if (c < 5) {
#pragma unroll
            for (int i = 0; i < 2; ++i) {
                const int r = 16 * wv + 8 * i + (l >> 3);
                uint32_t o = (uint32_t)((((64 * fa + r) * 514 + 64 * fkb) & ~15) + c * 16);
                o = o < lim ? o : lim;
                glds1(ldsb + L_SPARE + (2 * wv + i) * 1024, o, fr);
                glds1(ldsb + L_SPARE + 8192 + (2 * wv + i) * 1024, o, fi);
            }
        }
    };
    auto wt_out = [&]() {
        const char* dst = ws + WS_WT + (size_t)(32 * fkb) * 8192 + fa * 256;
#pragma unroll
        for (int i = 0; i < 3; ++i) {
            const int col = i < 2 ? 8 * wv + 4 * i + (l >> 4) : (fkb == 7 ? 32 : 8 * wv);
            u32x4 o;
#pragma unroll
            for (int c = 0; c < 4; ++c) {
                const int r = ((l & 15) >> 1) + 32 * (l & 1) + 8 * c, by = ((2 * r) & 15) + 2 * col;
                const uint32_t ad = (uint32_t)(L_SPARE + r * 128 + ((((by >> 4) ^ (r & 7) ^ 6) << 4) | (by & 15)));
                const uint32_t re = *(const uint16_t*)(lds + ad), im = *(const uint16_t*)(lds + ad + 8192);
                o[c] = re | (im << 16);
            }
            st16_wt(dst, (uint32_t)(col * 8192 + (l & 15) * 16), o);
        }
    };
    auto wt_flag = [&]() { if (l == 0) st8_wt(ws + WS_FW, (uint32_t)(((fkb * 4 + wv) * 32 + fa) * 8), (u32x2){tg0, tg1}); };

    if (__builtin_expect(!aux, 1)) {
        __builtin_amdgcn_s_barrier();
        stage(1);
        if (!fftw) wt_stage();
        ld8_dev(tagv, ws + WS_EP, 0u);
        if (EARLY2) stage(2);
        if (fftw) fft_twiddles();

        const int wsd = w & 1, wn = w >> 1;
        const uint32_t wa = hh * 4096 + ((l >> 2) & 3) * 64 + ((l >> 4) & 1) * 32 + (l & 3) * 8 + wn * 512;
        const uint32_t wa2 = wa + 49152;
        const uint32_t xa = A_XRING + (64 * wsd + l31) * 128 + ((hh ^ ((l31 >> 1) & 7)) << 4);
        f32x16 acc[2][2];
#pragma unroll
        for (int i = 0; i < 2; ++i)
#pragma unroll
            for (int j = 0; j < 2; ++j)
#pragma unroll
                for (int e = 0; e < 16; ++e) acc[i][j][e] = 0.f;
        u32x4 fw[LAG + 1][2], fx[LAG + 1][2];
        auto rd = [&](int it, int i) {
            const int buf = (4 * it + i) % (LAG + 1);
            const int sl = it % 3;
            const uint32_t wb = (i < 2 ? wa : wa2) + (i & 1) * 24576 + sl * 8192;
            fw[buf][0] = lds_tr2(lds, wb);
            fx[buf][0] = *(const u32x4*)(lds + (xa ^ (i * 32)) + sl * 16384);
            fx[buf][1] = *(const u32x4*)(lds + (xa ^ (i * 32)) + sl * 16384 + 4096);
            fw[buf][1] = lds_tr2(lds, wb + 2048);
        };
        auto mm = [&](int n) {
            if (n < 0) { __builtin_amdgcn_sched_barrier(0); return; }
            const int buf = n % (LAG + 1);
            acc[0][0] = mfma32(fw[buf][0], fx[buf][0], acc[0][0]);
            acc[0][1] = mfma32(fw[buf][0], fx[buf][1], acc[0][1]);
            acc[1][0] = mfma32(fw[buf][1], fx[buf][0], acc[1][0]);
            acc[1][1] = mfma32(fw[buf][1], fx[buf][1], acc[1][1]);
            __builtin_amdgcn_sched_group_barrier(0x8, 1, 0); __builtin_amdgcn_sched_group_barrier(0x100, 2, 0);
            __builtin_amdgcn_sched_group_barrier(0x8, 1, 0); __builtin_amdgcn_sched_group_barrier(0x100, 2, 0);
            __builtin_amdgcn_sched_group_barrier(0x8, 1, 0); __builtin_amdgcn_sched_group_barrier(0x100, 2, 0);
            __builtin_amdgcn_sched_group_barrier(0x8, 1, 0);
            asm volatile("" : "+v"(acc[0][0]), "+v"(acc[0][1]), "+v"(acc[1][0]), "+v"(acc[1][1]));
            __builtin_amdgcn_sched_barrier(0);
        };
        WAIT2(fftw, 7 + 6 * EARLY2, 11 + 6 * EARLY2);
        __builtin_amdgcn_s_barrier();
        asm volatile("" ::: "memory");
        TS(1);
        rd(0, 0);
        __builtin_amdgcn_sched_barrier(0);
        uint64_t ck0 = 0;
        u32x2 fl;
        const uint32_t sfo = (uint32_t)(((threadIdx.x & 2) ? 32 + 2 * mt + (threadIdx.x & 1) : nt + 14 * (threadIdx.x & 1)) * 8);
        u32x4 stv;
        u32x2 csv[2];
#pragma unroll
        for (int it = 0; it < 8; ++it) {
            rd(it, 1); mm(4 * it + 1 - LAG);
            if (it == 0 && !EARLY2) stage(2);
            if (PIECES && it >= 1 && it + 2 < 8) { piece(it + 2, 2); piece(it + 2, 3); }
            if (it == 3 && fftw) fft_consts();
            if (it == 6) wdstages(5, 8);
            if (it == 7) { ld8_dev_w0(w >> 2, wfl, ws + WS_FW, flo); wdstages(10, 12); }
            rd(it, 2); mm(4 * it + 2 - LAG);
            if (PIECES && it >= 1 && it + 2 < 8) { piece(it + 2, 4); piece(it + 2, 5); }
            if (it == 1) touch((uint32_t)(((g * 28 + gi) * 8 + w) * 8 + (l & 7)) * 128, wd);
            if (it == 1 && !fftw) wt_out();
            if (it == 5) wdstages(0, 2);
            if (it == 6) ld8_dev_w0(w, fl, ws + WS_FA, sfo);
            rd(it, 3); mm(4 * it + 3 - LAG);
            if (it < 7) {
                if (it == 1 || it == 2) WAIT2(fftw, 7, 10);
                else if (it == 5) WAIT2(fftw, 6, 10);
                else if (it == 6) {
                    WAIT2(fftw, 0, 16);
                    asm volatile("" : "+v"(fl));
                    if (w == 0) poll_flags(fl, ws + WS_FA, sfo, tg0, tg1);
                }
                else asm volatile("s_waitcnt vmcnt(6) lgkmcnt(0)" ::: "memory");
                __builtin_amdgcn_s_barrier();
                asm volatile("" ::: "memory");
                if (it == 2) TS(3);
                if (it == 5) TS(5);
                if (it == 6) {
                    TS(6);
                    if (SCALAR_EPI && fftw) __builtin_amdgcn_s_setprio(2);
                    ld16_dev(stv, ws + WS_ST + (128 * mt + 64 * wsd) * 8, (uint32_t)(l31 * 16));
#pragma unroll
                    for (int gu = 0; gu < 2; ++gu) ld8_dev(csv[gu], ws + WS_CS + (gu * 1792 + 128 * nt + 32 * wn) * 8, (uint32_t)(l31 * 8));
                }
                rd(it + 1, 0);
                mm(4 * it + 4 - LAG);
            }
            if (it == 3 && !fftw) wt_flag();
            if (it + 3 < 8) { if (PIECES) { piece(it + 3, 0); piece(it + 3, 1); } else stage(it + 3); }
            if (it == 0) tag_set();
            if (it == 4) refresh();
            if (it == 5) wdstages(2, 5);
            if (it == 6) {
                if (tid < 16) *(volatile LDSP uint32_t*)(lds + L_SYNC + tid * 4) = 0u;
                wdstages(8, 10);
            }
        }
#pragma unroll
        for (int n = 32 - LAG; n < 31; ++n) mm(n);
        if (fftw) asm volatile("s_waitcnt vmcnt(0)" ::: "memory"); else asm volatile("s_waitcnt vmcnt(8)" ::: "memory");
        asm volatile("" : "+v"(stv), "+v"(csv[0]), "+v"(csv[1]), "+v"(wfl));
        early = __builtin_amdgcn_readfirstlane(__builtin_amdgcn_ballot_w64(wfl[0] != tg0 || wfl[1] != tg1) == 0ull ? fsel : 0);
        e_loads(0, early, 0); e_loads(4, early, 0); w_loads(early, 0);
        mm(31);
        ld8_dev_w0(early ^ fsel ^ 1, wfl, ws + WS_FW, flo);
#pragma unroll
        for (int st = 0; st < 2; ++st)
#pragma unroll
            for (int gu = 0; gu < 2; ++gu) {
                const u32x4 ca = {hh ? 0u : csv[gu][0], hh ? 0u : csv[gu][1], 0u, 0u};
                const uint32_t nm = stv[2 * st];
                acc[gu][st] = mfma32(ca, (u32x4){nm, nm, 0u, 0u}, acc[gu][st]);
            }
        asm volatile("" : "+v"(acc[0][0]), "+v"(acc[0][1]), "+v"(acc[1][0]), "+v"(acc[1][1]));
        TS(2);
        asm volatile("s_nop 15" ::: "memory");
        const char* hp = ws + WS_H + (size_t)((2 * mt + wsd) * 112 + 8 * nt + 2 * wn) * 2048;
#pragma unroll
        for (int st = 0; st < 2; ++st) {
            const uint32_t riu = stv[2 * st + 1];
            const float r2 = __builtin_bit_cast(float, riu);
#pragma unroll
            for (int u = 0; u < 2; ++u) {
                u32x4 ho;
#pragma unroll
                for (int e = 0; e < 4; ++e) {
                    const int r0 = 8 * u + 2 * e, r1 = r0 + 1;
                    float g0, g1;
                    const float a0 = acc[0][st][r0], a1 = acc[0][st][r1], b0 = acc[1][st][r0], b1 = acc[1][st][r1];
                    asm("v_max_f32 %0, 0, %2\n\tv_max_f32 %1, 0, %3\n\tv_mul_f32 %0, %0, %6\n\tv_mul_f32 %1, %1, %6\n\tv_mul_f32 %0, %0, %4\n\tv_mul_f32 %1, %1, %5"
                        : "=&v"(g0), "=&v"(g1) : "v"(a0), "v"(a1), "v"(b0), "v"(b1), "v"(r2));
                    ho[e] = pk_bf16(g0, g1);
                }
                st16_wt(hp, (uint32_t)(u * 2048 + st * 1024 + l * 16), ho);
            }
        }
        if (fftw) WAITV(4);
        asm volatile("" : "+v"(wfl));
        {
            const int mid = __builtin_amdgcn_readfirstlane(__builtin_amdgcn_ballot_w64(wfl[0] != tg0 || wfl[1] != tg1) == 0ull ? (early ^ fsel) : 0);
            e_loads(0, mid, 1); e_loads(4, mid, 1); w_loads(mid, 1);
            asm volatile("s_cmp_eq_u32 %0, 0\n\ts_cbranch_scc1 1f\n\ts_waitcnt vmcnt(0)\n1:" :: "s"(mid) : "memory", "scc");
            early |= mid;
        }
        tie_fft(false);
    } else {
        const int a = 8 * (g & 3) + 4 * (g >> 2) + idx;
        if (tid < 16) *(volatile LDSP uint32_t*)(lds + L_SYNC + tid * 4) = 0u;
        auto arrive = [&](int word) { asm volatile("" ::: "memory"); if (l == 0) __hip_atomic_fetch_add((LDSP uint32_t*)(lds + L_SYNC) + (word >> 2), 1u, __ATOMIC_RELAXED, __HIP_MEMORY_SCOPE_WORKGROUP); };
        {
            const int reg = 4 * (l31 >> 3) + (l31 & 3), part = reg & 1;
#pragma unroll
            for (int nu = 0; nu < 4; ++nu) {
                const int kb = 16 * nu + 8 * ((l31 >> 2) & 1) + (reg >> 1);
                const bool nyq = kb == 0 && part == 1;
                const float step = nyq ? 0.5f : (float)kb * (1.0f / 128.0f), ph = (part && !nyq) ? 0.25f : 0.f;
                const float c2 = 2.0f * __builtin_amdgcn_cosf(step);
                const float a0 = __builtin_amdgcn_fractf(__builtin_fmaf((float)(16 * w + 8 * hh + 3), step, ph));
                float c[8];
                c[3] = __builtin_amdgcn_cosf(a0); c[4] = __builtin_amdgcn_cosf(a0 + step);
                c[5] = __builtin_fmaf(c2, c[4], -c[3]); c[2] = __builtin_fmaf(c2, c[3], -c[4]);
                c[6] = __builtin_fmaf(c2, c[5], -c[4]); c[1] = __builtin_fmaf(c2, c[2], -c[3]);
                c[7] = __builtin_fmaf(c2, c[6], -c[5]); c[0] = __builtin_fmaf(c2, c[1], -c[2]);
                *(u32x4*)(lds + X_TF + (w * 4 + nu) * 1024 + lo) = (u32x4){pk_bf16(c[0], c[1]), pk_bf16(c[2], c[3]), pk_bf16(c[4], c[5]), pk_bf16(c[6], c[7])};
            }
        }
        if (fftw) {
            uint32_t onel = 0x00003f80u, oneh = 0x3f800000u;
            asm volatile("" : "+v"(onel), "+v"(oneh));
            const int ca = a < 28 ? a : 27;
            const char* csrc = (const char*)(ca < 14 ? wg : wu) + 256 * (ca < 14 ? ca : ca - 14);
            const uint32_t cofs = (uint32_t)((t >> 4) * 3584 + (t & 15) * 16);
            u32x4 cw[32];
            ld8_dev(tagv, ws + WS_EP, 0u);
#pragma unroll
            for (int i = 0; i < 8; ++i) ld16(cw[i], csrc + (size_t)i * 16 * 3584, cofs);
            WAITV(8);
            BARRIER();
            tag_set();
            TS(1);
#pragma unroll
            for (int i = 8; i < 32; ++i) ld16(cw[i], csrc + (size_t)i * 16 * 3584, cofs);
            float c[8];
#pragma unroll
            for (int e = 0; e < 8; ++e) c[e] = 0.f;
#pragma unroll
            for (int i = 0; i < 32; ++i) {
                if ((i & 3) == 0) asm volatile("s_waitcnt vmcnt(%4)" : "+v"(cw[i]), "+v"(cw[i + 1]), "+v"(cw[i + 2]), "+v"(cw[i + 3]) : "i"(28 - i) : "memory");
#pragma unroll
                for (int e = 0; e < 4; ++e) { const uint32_t v = cw[i][e]; c[2 * e] = dot2u(v, onel, c[2 * e]); c[2 * e + 1] = dot2u(v, oneh, c[2 * e + 1]); }
            }
            *(f32x4*)(lds + X_CS + (t >> 4) * 512 + (t & 15) * 32) = (f32x4){c[0], c[1], c[2], c[3]};
            *(f32x4*)(lds + X_CS + (t >> 4) * 512 + (t & 15) * 32 + 16) = (f32x4){c[4], c[5], c[6], c[7]};
            arrive(52);
            TS(2);
            if (w == 0) {
                lds_wait_ge<1>(lds, L_SYNC + 52, 4);
                f32x2 s0 = {0.f, 0.f}, s1 = {0.f, 0.f};
#pragma unroll
                for (int i = 0; i < 16; i += 2) { s0 += *(const f32x2*)(lds + X_CS + i * 512 + l * 8); s1 += *(const f32x2*)(lds + X_CS + (i + 1) * 512 + l * 8); }
                const f32x2 cs = s0 + s1;
                const float a0 = rbf(cs.x), a1 = rbf(cs.x - a0), b0 = rbf(cs.y), b1 = rbf(cs.y - b0);
                if (a < 28) st16_wt(ws + WS_CS, (uint32_t)(a * 1024 + l * 16), (u32x4){pk_bf16(a0, a1), pk_bf16(cs.x - a0 - a1, 0.f), pk_bf16(b0, b1), pk_bf16(cs.y - b0 - b1, 0.f)});
                WAITV(0);
                if (l == 0) st8_wt(ws + WS_FA, (uint32_t)(a * 8), (u32x2){tg0, tg1});
            }
            TS(3);
            lds_wait_ge<1>(lds, L_SYNC + 56, 4);
            refresh();
            wt_out();
            fft_twiddles();
            WAITV(0);
            wt_flag();
            fft_consts();
        } else {
            ld8_dev(tagv, ws + WS_EP, 0u);
            wt_stage();
            f32x16 acc[2][4];
#pragma unroll
            for (int ct = 0; ct < 2; ++ct)
#pragma unroll
                for (int r = 0; r < 4; ++r)
#pragma unroll
                    for (int e = 0; e < 16; ++e) acc[ct][r][e] = 0.f;
            float sq[2] = {0.f, 0.f};
            const int rho = 4 * ((l >> 4) & 1) + (l & 3);
            const uint32_t xa = (uint32_t)(wv * 16384 + rho * 1024 + ((64 * hh + 8 * ((l >> 2) & 3)) ^ (rho << 5)));
            WAITV(4);
            BARRIER();
            __builtin_amdgcn_s_setprio(3);
            tag_set();
            TS(1);
#pragma unroll
            for (int kap = 0; kap < 8; ++kap) {
                u32x4 xf[2];
#pragma unroll
                for (int ct = 0; ct < 2; ++ct) {
                    bf16x4 p0 = __builtin_amdgcn_ds_read_tr16_b64_v4bf16((LDSP bf16x4*)(lds + (xa ^ ((kap & 1) * 128)) + (kap >> 1) * 256 + ct * 8192));
                    bf16x4 p1 = __builtin_amdgcn_ds_read_tr16_b64_v4bf16((LDSP bf16x4*)(lds + (xa ^ ((kap & 1) * 128 + 32)) + (kap >> 1) * 256 + ct * 8192));
                    xf[ct] = __builtin_bit_cast(u32x4, __builtin_shufflevector(p0, p1, 0, 1, 2, 3, 4, 5, 6, 7));
                }
#pragma unroll
                for (int nu = 0; nu < 4; ++nu) {
                    const u32x4 tf = *(const u32x4*)(lds + X_TF + (kap * 4 + nu) * 1024 + lo);
                    acc[0][nu] = mfma32(tf, xf[0], acc[0][nu]);
                    acc[1][nu] = mfma32(tf, xf[1], acc[1][nu]);
                }
#pragma unroll
                for (int ct = 0; ct < 2; ++ct)
#pragma unroll
                    for (int e = 0; e < 4; ++e) { const uint32_t v = xf[ct][e]; sq[ct] = dot2u(v, v, sq[ct]); }
            }
            TS(2);
            float rs[2];
#pragma unroll
            for (int ct = 0; ct < 2; ++ct) {
                float sx = hh ? 0.f : acc[ct][0][0], s2 = sq[ct];
                sx += dppf<0xB1>(sx); s2 += dppf<0xB1>(s2);
                sx += dppf<0x4E>(sx); s2 += dppf<0x4E>(s2);
                sx += __shfl_xor(sx, 32); s2 += __shfl_xor(s2, 32);
                const int s = 64 * a + 16 * wv + 8 * ct + (l31 >> 2);
                const float mu = sx * (1.0f / 512.0f);
                float vr = s2 * (1.0f / 512.0f) - mu * mu;
                if (__builtin_amdgcn_ballot_w64(mu * mu > 256.0f * vr) != 0ull) vr = row_var_exact(x + (size_t)s * 512 + (hh * 4 + (l & 3)) * 64, mu);
                float mb;
                ln_finish(mu, vr, mb, rs[ct]);
                acc[ct][0][0] -= hh ? 0.f : 128.0f * mb;
                st8_wt(ws + WS_ST, (uint32_t)(s * 8), (u32x2){pk_bf16(-mb, -mb), __builtin_bit_cast(uint32_t, rs[ct] * rs[ct])});
            }
            const uint32_t eo = (uint32_t)(hh * 8 * 32768 + l31 * 4 + (64 * a + 16 * wv) * 16);
#pragma unroll
            for (int nu = 0; nu < 4; ++nu) {
#pragma unroll
                for (int i = 0; i < 8; ++i) {
                    const char* eb = ws + WS_E + (size_t)(16 * nu + i) * 32768;
                    asm volatile("global_store_dword %0, %1, %3 sc1\n\tglobal_store_dword %0, %2, %3 offset:128 sc1"
                                 :: "v"(eo), "v"(pk_f16(acc[0][nu][2 * i] * rs[0], acc[0][nu][2 * i + 1] * rs[0])), "v"(pk_f16(acc[1][nu][2 * i] * rs[1], acc[1][nu][2 * i + 1] * rs[1])), "s"(eb) : "memory");
                }
                if (nu == 0) { WAITV(16); arrive(56); }
                if (nu == 1 && w == 7) { lds_wait_ge<1>(lds, L_SYNC + 56, 4); if (l == 0) st8_wt(ws + WS_FA, (uint32_t)(256 + a * 8), (u32x2){tg0, tg1}); }
            }
            TS(3);
            __builtin_amdgcn_s_setprio(0);
            WAITV(0);
            arrive(60);
            if (w == 4) { lds_wait_ge<1>(lds, L_SYNC + 60, 4); if (l == 0) st8_wt(ws + WS_FA, (uint32_t)(512 + a * 8), (u32x2){tg0, tg1}); }
            TS(5);
        }
        refresh();
        BARRIER();
        if (!fftw) lds_wait_ge<4>(lds, L_SYNC + 48, 1);
        wdstages(0, 12);
#pragma unroll
        for (int i = 0; i < 8; ++i) { ein[i] = (u32x4){0u, 0u, 0u, 0u}; wt[i] = 0u; }
    }
    TS(7);

    const uint32_t hflag = (uint32_t)((2 * mt + (w & 1)) * 8192 + (nt * 4 + (w >> 1)) * 128);
    refresh();
    auto fsync = [&](uint32_t target) {
        asm volatile("" ::: "memory");
        if (l == 0) __hip_atomic_fetch_add((LDSP uint32_t*)(lds + L_SYNC), 1u, __ATOMIC_RELAXED, __HIP_MEMORY_SCOPE_WORKGROUP);
        lds_wait_ge(lds, L_SYNC, target);
    };
    auto pplace = [&](bool dn, int u, int q) -> uint32_t {
        if (!dn) return (uint32_t)(u * 24576 + 8192 + q * 1024);
        return q >= 8 ? (uint32_t)(u * 24576 + (q - 8) * 1024)
                      : (uint32_t)((q < 4 ? L_SPARE : A_XRING + 32768 - 4096) + u * 4096 + q * 1024);
    };
    auto pwrite = [&](bool dn, f32x16 (&acc)[2][2]) {
#pragma unroll
        for (int q = 0; q < 16; ++q) {
            const f32x16& a = acc[q >> 3][(q >> 2) & 1];
            *(f32x4*)(lds + pplace(dn, wv, q) + ((l ^ ((q & 7) << 1) ^ hh) << 4)) = (f32x4){a[4 * (q & 3)], a[4 * (q & 3) + 1], a[4 * (q & 3) + 2], a[4 * (q & 3) + 3]};
        }
    };
    f32x16 acc[2][2];
    u32x2 xres[2];
    auto xload = [&]() {
        const uint32_t o = (uint32_t)(((64 * sp + 8 * w + 2 * (l >> 4)) * 512 + 64 * dp + (l & 15) * 4) * 2);
        asm volatile("global_load_dwordx2 %0, %1, %2" : "=v"(xres[0]) : "v"(o), "s"(x) : "memory");
        asm volatile("global_load_dwordx2 %0, %1, %2 offset:1024" : "=v"(xres[1]) : "v"(o), "s"(x) : "memory");
    };
    if (fftw) {
        f32x2* buf = (f32x2*)(lds + L_FBUF);
        __builtin_amdgcn_s_setprio(2);
#pragma unroll
        for (int i = 0; i < 7; ++i) { const f32x4 q = __builtin_bit_cast(f32x4, ev[i]); twA[i] = q.xy; twB[i] = q.zw; }
        if (!early) {
            if (!aux) BARRIER();
            WAITV(0);
            if (!aux && l == 0) st8_wt(ws + WS_FH, hflag, (u32x2){tg0, tg1});
            u32x2 nfl;
            ld8_dev(nfl, ws + WS_FW, k == 0 ? (uint32_t)((31 * 32 + l31) * 8) : flo);
            poll_now(ws + WS_FW, flo, tg0, tg1);
            asm volatile("" : "+v"(nfl));
            if (k == 0) poll_flags(nfl, ws + WS_FW, (uint32_t)((31 * 32 + l31) * 8), tg0, tg1);
        }
        e_loads(0, early ^ 1, 1); e_loads(4, early ^ 1, 1); w_loads(early ^ 1, 1);
        asm volatile(WLD : "=&v"(w3[0]), "=&v"(w3[1]), "=&v"(w3[2]), "=&v"(w3[3]), "=&v"(w3[4]), "=&v"(w3[5]), "=&v"(w3[6]), "=&v"(w3[7])
                     : "v"((uint32_t)(fm * 2048 + (fp * 8 + fq) * 4)), "s"(ws + WS_WT + 256 * 8192), "s"((b - 1) >> 31) : "memory", "scc");
        asm volatile("s_cmp_lg_u32 %0, 0\n\ts_cbranch_scc1 1f\n\ts_waitcnt vmcnt(0)\n1:" :: "s"(early) : "memory", "scc");
        tie_fft(false);
        if (aux) *(volatile LDSP uint32_t*)(lds + L_SYNC + 48) = 1u;
        const bool low0 = (fp | fq) == 0;
        const uint32_t mir7 = (uint32_t)(L_FBUF + (514 * (3 - fm) + 64 - fp - 8 * fq) * 8), mir0 = low0 ? (uint32_t)(L_FBUF + 514 * ((4 - fm) & 3) * 8) : mir7 + 7 * 512;
        if (k == 0) {
#pragma unroll
            for (int i = 0; i < 8; ++i) {
                asm volatile("" : "+v"(w3[i]));
                *(u32x2*)(lds + L_FBUF + (fp + 8 * fq + 514 * fm + 64 * i) * 8) = (u32x2){wt[i], w3[i]};
            }
            asm volatile("" ::: "memory");
            if (l == 0) __hip_atomic_fetch_add((LDSP uint32_t*)(lds + L_SYNC + 24), 1u, __ATOMIC_RELAXED, __HIP_MEMORY_SCOPE_WORKGROUP);
            const bool ri = fm == 0 || fm == 3;
            const f32x2 cg = {ri ? 0.f : 1.f, ri ? 1.f : 0.f};
            const f32x2 c0 = low0 ? (f32x2){(fm & 1) ? 0.f : 1.f, (fm & 1) ? -1.f : 0.f} : cg;
            lds_wait_ge(lds, L_SYNC + 24, 4);
#pragma unroll
            for (int i = 0; i < 8; ++i) {
                const u32x2 mv = *(const u32x2*)(lds + (i == 0 ? mir0 : mir7 + (7 - i) * 512));
                const uint32_t m0_ = mv[0], m1_ = mv[1];
                const f32x2 P = {bf_lo(wt[i]) + bf_lo(m0_), bf_hi(wt[i]) - bf_hi(m0_)}, Q = {bf_lo(w3[i]) + bf_lo(m1_), bf_hi(w3[i]) - bf_hi(m1_)};
                wv2[i] = P - Q;
                uq[i] = P + Q;
            }
            asm volatile("" ::: "memory");
            if (l == 0) __hip_atomic_fetch_add((LDSP uint32_t*)(lds + L_SYNC + 24), 1u, __ATOMIC_RELAXED, __HIP_MEMORY_SCOPE_WORKGROUP);
            cmul4<false>(wv2[0], wv2[1], wv2[2], wv2[3], c0, cg, cg, cg);
            cmul4<false>(wv2[4], wv2[5], wv2[6], wv2[7], cg, cg, cg, cg);
        }
        f32x2 xv[8];
        {
#pragma unroll
            for (int a = 0; a < 8; ++a) {
                float re = 0.f, im = 0.f;
#pragma unroll
                for (int r = 0; r < 4; ++r) {
                    const uint32_t e = ein[a][r];
                    re = __builtin_amdgcn_fdot2(__builtin_bit_cast(f16x2, e), __builtin_bit_cast(f16x2, cr[r]), re, false);
                    im = __builtin_amdgcn_fdot2(__builtin_bit_cast(f16x2, e), __builtin_bit_cast(f16x2, ci[r]), im, false);
                }
                xv[a] = (f32x2){re, im};
            }
        }
        for (int i = 0; i < 8; ++i) asm volatile("" : "+v"(xv[i]));
        FS(1);
        const float sg = fv < 2 ? 1.f : -1.f, cc = fv == 1 ? -1.f : 1.f;
        dft8<-1>(xv);
        twiddle7<false>(xv, twA);
        if (early) BARRIER();
        if (k == 0) lds_wait_ge(lds, L_SYNC + 24, 8);
#pragma unroll
        for (int i = 0; i < 8; ++i) buf[i * 288 + t] = xv[i];
        asm volatile("" ::: "memory");
        if (l == 0) __hip_atomic_fetch_add((LDSP uint32_t*)(lds + L_SYNC), 1u, __ATOMIC_RELAXED, __HIP_MEMORY_SCOPE_WORKGROUP);
        lds_wait_ge(lds, L_SYNC, 4);
#pragma unroll
        for (int i = 0; i < 8; ++i) xv[i] = buf[fp * 288 + fu + 32 * i];
        FS(3);
        dft8<-1>(xv);
        twiddle7<false>(xv, twB);
#pragma unroll
        for (int i = 0; i < 8; ++i) buf[fp * 288 + i * 36 + fu] = xv[i];
        __builtin_amdgcn_wave_barrier();
#pragma unroll
        for (int i = 0; i < 8; ++i) xv[i] = buf[fp * 288 + fq * 36 + fv + 4 * i];
        WAITV(0);
        if (!aux && l == 0) st8_wt(ws + WS_FH, hflag, (u32x2){tg0, tg1});
        dft8<-1>(xv);
        twiddle7<false>(xv, twC);
        quad4<false>(xv[0], xv[1], xv[2], xv[3], sg, cc);
        quad4<false>(xv[4], xv[5], xv[6], xv[7], sg, cc);
        FS(5);
        FS(6);
        if (k == 0) {
            const int j0 = fp + 8 * fq + 514 * fm;
            fsync(8);
#pragma unroll
            for (int i = 0; i < 8; ++i) buf[j0 + 64 * i] = xv[i];
            asm volatile("" ::: "memory");
            if (l == 0) __hip_atomic_fetch_add((LDSP uint32_t*)(lds + L_SYNC), 1u, __ATOMIC_RELAXED, __HIP_MEMORY_SCOPE_WORKGROUP);
            cmul4<false>(xv[0], xv[1], xv[2], xv[3], uq[0], uq[1], uq[2], uq[3]);
            cmul4<false>(xv[4], xv[5], xv[6], xv[7], uq[4], uq[5], uq[6], uq[7]);
            lds_wait_ge(lds, L_SYNC, 12);
            f32x2 o[8];
#pragma unroll
            for (int i = 0; i < 8; ++i) o[i] = *(const f32x2*)(lds + (i == 0 ? mir0 : mir7 + (7 - i) * 512));
            asm volatile("" ::: "memory");
            if (l == 0) __hip_atomic_fetch_add((LDSP uint32_t*)(lds + L_SYNC), 1u, __ATOMIC_RELAXED, __HIP_MEMORY_SCOPE_WORKGROUP);
            cmul4<true>(wv2[0], wv2[1], wv2[2], wv2[3], o[0], o[1], o[2], o[3]);
            cmul4<true>(wv2[4], wv2[5], wv2[6], wv2[7], o[4], o[5], o[6], o[7]);
#pragma unroll
            for (int i = 0; i < 8; ++i) xv[i] += wv2[i];
        } else {
            f32x2 wf[8];
#pragma unroll
            for (int i = 0; i < 8; ++i) wf[i] = (f32x2){bf_lo(wt[i]), bf_hi(wt[i])};
            cmul4<false>(xv[0], xv[1], xv[2], xv[3], wf[0], wf[1], wf[2], wf[3]);
            cmul4<false>(xv[4], xv[5], xv[6], xv[7], wf[4], wf[5], wf[6], wf[7]);
        }
#define RELEASE() do { _Pragma("unroll") for (int i_ = 0; i_ < 8; ++i_) asm volatile("" : "+v"(xv[i_])); *(volatile LDSP uint32_t*)(lds + L_SYNC + 32 + w * 4) = 1u; } while (0)
        RELEASE();
        quad4<true>(xv[0], xv[1], xv[2], xv[3], -sg, -cc);
        quad4<true>(xv[4], xv[5], xv[6], xv[7], -sg, -cc);
        twiddle7s<true>(xv, twC);
        dft8s<1>(xv);

        if (k == 0) lds_wait_ge(lds, L_SYNC, 16);
#pragma unroll
        for (int i = 0; i < 8; ++i) buf[fp * 288 + fq * 36 + fv + 4 * i] = xv[i];
        __builtin_amdgcn_wave_barrier();
#pragma unroll
        for (int i = 0; i < 8; ++i) xv[i] = buf[fp * 288 + i * 36 + fu];
        twiddle7s<true>(xv, twB);
        dft8s<1>(xv);
#pragma unroll
        for (int i = 0; i < 8; ++i) buf[fp * 288 + fu + 32 * i] = xv[i];
        FS(14);
        fsync(k == 0 ? 20 : 8);
#pragma unroll
        for (int i = 0; i < 8; ++i) xv[i] = buf[i * 288 + t];
        twiddle7s<true>(xv, twA);
        dft8s<1>(xv);
        const float zsc = k == 0 ? 1.0f / 4194304.0f : 1.0f / 524288.0f;
        TS(4);
#pragma unroll
        for (int a = 0; a < 8; ++a) st4_wt(ws + WS_Z + (size_t)k * 8192 + a * 1024, (uint32_t)(t * 4), pk_bf16(smul(xv[a].x, zsc), smul(xv[a].y, zsc)));
        u32x4 gf[4][4];
#pragma unroll
        for (int dq = 0; dq < 4; ++dq) {
            const int d = 64 * dp + 16 * dq + (l & 15);
            const float step = (float)d * (1.0f / 512.0f), c2 = 2.0f * __builtin_amdgcn_cosf(step);
#pragma unroll
            for (int kap = 0; kap < 4; ++kap) {
                const float a = __builtin_amdgcn_fractf((float)(64 * w + 4 * (l >> 4) + 16 * kap + 1) * step), a2 = a + step;
                const float c1 = __builtin_amdgcn_cosf(a), s1 = __builtin_amdgcn_sinf(-a), cc2 = __builtin_amdgcn_cosf(a2), s2 = __builtin_amdgcn_sinf(-a2);
                float s0 = sfms(s1, c2, s2);
                if (kap == 0) s0 = (w == 0 && l < 16) ? ((d & 1) ? -1.f : 1.f) : s0;
                gf[kap][dq] = (u32x4){pk_bf16(sfms(c1, c2, cc2), s0), pk_bf16(c1, s1), pk_bf16(cc2, s2), pk_bf16(sfms(cc2, c2, c1), sfms(s2, c2, s1))};
                asm volatile("" : "+v"(gf[kap][dq]));
            }
            if (dq == ZDQ) {
                WAITV(0);
                if (l == 0) st8_wt(ws + WS_FZ, (uint32_t)((w * 256 + k) * ZFS), (u32x2){tg0, tg1});
                __builtin_amdgcn_s_setprio(0);
                TS(8);
            }
        }
        lds_wait_ge<4>(lds, L_SYNC + 4 + w * 4, 1);
        {
            const uint32_t zfo = (uint32_t)(((sp & 3) * 256 + 64 * w + l) * ZFS);
            u32x2 zfl;
            asm volatile("global_load_dwordx2 %0, %1, %2 sc1\n\ts_waitcnt vmcnt(0)" : "=&v"(zfl) : "v"(zfo), "s"(ws + WS_FZ) : "memory");
            poll_flags(zfl, ws + WS_FZ, zfo, tg0, tg1);
        }
        TS(9);
        xload();
        u32x4 zl[4][4];
        {
            const char* zB = ws + WS_Z + (size_t)(64 * w) * 8192 + sp * 256;
            const uint32_t zo = (uint32_t)((l >> 4) * 4 * 8192 + (l & 15) * 16);
#pragma unroll
            for (int kap = 0; kap < 4; ++kap)
#pragma unroll
                for (int e = 0; e < 4; ++e) ld16_dev(zl[kap][e], zB + (size_t)(16 * kap + e) * 8192, zo);
        }
        TS(10);
        f32x4 za[4][4];
#pragma unroll
        for (int i = 0; i < 4; ++i)
#pragma unroll
            for (int j = 0; j < 4; ++j) za[i][j] = (f32x4){0.f, 0.f, 0.f, 0.f};
#pragma unroll
        for (int kap = 0; kap < 4; ++kap) {
            asm volatile("s_waitcnt vmcnt(%4)" : "+v"(zl[kap][0]), "+v"(zl[kap][1]), "+v"(zl[kap][2]), "+v"(zl[kap][3]) : "i"(4 * (3 - kap)) : "memory");
#pragma unroll
            for (int si = 0; si < 4; ++si) {
                const u32x4 zf = {zl[kap][0][si], zl[kap][1][si], zl[kap][2][si], zl[kap][3][si]};
#pragma unroll
                for (int dq = 0; dq < 4; ++dq)
                    za[si][dq] = __builtin_amdgcn_mfma_f32_16x16x32_bf16(__builtin_bit_cast(bf16x8, gf[kap][dq]), __builtin_bit_cast(bf16x8, zf), za[si][dq], 0, 0, 0);
            }
        }
        TS(11);
        asm volatile("" : "+v"(xres[0]), "+v"(xres[1]));
        lds_wait_ge<1>(lds, L_SYNC + 4 + w * 4, 2);
#pragma unroll
        for (int si = 0; si < 4; ++si)
#pragma unroll
            for (int dq = 0; dq < 4; ++dq) {
                const int q3 = 2 * dq + hh, h2 = (l >> 4) & 1, col = 2 * (l & 15) + (si >> 1);
                *(f32x4*)(lds + pplace(false, wv, 8 * (si & 1)) + q3 * 1024 + (((h2 * 32 + col) ^ (q3 << 1) ^ h2) << 4)) = za[si][dq];
            }
    }
    int wq = w;
    asm volatile("" : "+s"(wq));
    if (wq >= 4) {
        refresh();
        if (!aux) BARRIER();
#pragma unroll
        for (int i = 12; i < 16; ++i) wdstage(i);
        WAITV(8);
        if (!aux && l == 0) st8_wt(ws + WS_FH, hflag, (u32x2){tg0, tg1});
        TS(4);
        u32x4 wdr[12][2];
        {
            const uint32_t fo = (uint32_t)(sp * 8192 + (14 * wv + (l < 14 ? l : 13)) * 128);
            u32x2 hfl;
#pragma unroll
            for (int i = 0; i < 12; ++i) { wdr[i][0] = wdread(i, 0); wdr[i][1] = wdread(i, 1); }
#pragma unroll
            for (int i = 0; i < RPRE; ++i) { asm volatile("" : "+v"(wdr[i][0]), "+v"(wdr[i][1])); wdstage(16 + i); }
            TS(8);
            asm volatile("global_load_dwordx2 %0, %1, %2 sc1\n\ts_waitcnt vmcnt(0)" : "=&v"(hfl) : "v"(fo), "s"(ws + WS_FH) : "memory");
            poll_flags(hfl, ws + WS_FH, fo, tg0, tg1);
#pragma unroll
            for (int i = RPRE; i < 12; ++i) asm volatile("" : "+v"(wdr[i][0]), "+v"(wdr[i][1]));
        }
        TS(9);
        const char* hB = ws + WS_H + (size_t)(sp * 112 + 28 * wv) * 2048;
        u32x4 hf[28][2];
        auto hload1 = [&](int i, int st) {
            asm volatile("global_load_dwordx4 %0, %1, %2 offset:%3 sc1" : "=v"(hf[i][st]) : "v"(lo), "s"(hB + (ABL_HOT ? 0 : (i & ~1) * 2048)), "i"((i & 1) * 2048 + st * 1024) : "memory");
        };
        auto hload = [&](int i) { hload1(i, 0); hload1(i, 1); };
        constexpr int HD = HDEPTH;
#pragma unroll
        for (int i = 0; i < HD; ++i) hload(i);
#pragma unroll
        for (int i = 0; i < 2; ++i)
#pragma unroll
            for (int j = 0; j < 2; ++j)
#pragma unroll
                for (int e = 0; e < 16; ++e) acc[i][j][e] = 0.f;
        u32x4 wl[2][2];
        lds_wait_ge<2>(lds, L_SYNC + 32 + wv * 4, 1);
#define SB() __builtin_amdgcn_sched_barrier(0)
#pragma unroll
        for (int i = 0; i < 28; ++i) {
            const int j0_ = i - HD < 0 ? 0 : i - HD, j1_ = i < 12 - RPRE ? i : 12 - RPRE;
            const int nh = (i + HD - 1 < 27 ? i + HD - 1 : 27) - i + (j1_ > j0_ ? j1_ - j0_ : 0);
            if (i + 1 >= 12 && i + 1 < 28) { wl[(i + 1) & 1][0] = wdread(i + 1, 0); wl[(i + 1) & 1][1] = wdread(i + 1, 1); }
            if (i == ZGO || i == 20) { asm volatile("" ::: "memory"); *(volatile LDSP uint32_t*)(lds + L_SYNC + 4 + wv * 4) = i == 20 ? 2u : 1u; }
            SB();
            asm volatile("s_waitcnt vmcnt(%2)" : "+v"(hf[i][0]), "+v"(hf[i][1]) : "i"(2 * nh) : "memory");
            if (i == 0) TS(10);
            if (i == 12) TS(14);
            const u32x4 wa0 = i < 12 ? wdr[i][0] : wl[i & 1][0], wa1 = i < 12 ? wdr[i][1] : wl[i & 1][1];
            acc[0][0] = mfma32(wa0, hf[i][0], acc[0][0]);
            SB();
            if (i + HD < 28) hload1(i + HD, 0);
            SB();
            acc[0][1] = mfma32(wa1, hf[i][0], acc[0][1]);
            SB();
            if (i + HD < 28) hload1(i + HD, 1);
            SB();
            acc[1][0] = mfma32(wa0, hf[i][1], acc[1][0]);
            acc[1][1] = mfma32(wa1, hf[i][1], acc[1][1]);
            asm volatile("" : "+v"(acc[0][0]), "+v"(acc[0][1]), "+v"(acc[1][0]), "+v"(acc[1][1]));
            SB();
            if (i < 12 - RPRE) wdstage(16 + RPRE + i);
        }
        TS(11);
        xload();
        pwrite(true, acc);
        asm volatile("s_waitcnt vmcnt(0)" : "+v"(xres[0]), "+v"(xres[1]) :: "memory");
    }
    TS(12);
    refresh();
    const int oc = 4 * w + (l >> 4), oq = (l >> 1) & 7, ohh = l & 1;
    const uint32_t ooff = (uint32_t)(((64 * sp + 2 * oc) * 512 + 64 * dp + (l & 15) * 4) * 2);
    BARRIER();
    TS(13);
    {
        const uint32_t ro = (uint32_t)(oq * 1024 + (((ohh * 32 + oc) ^ (oq << 1) ^ ohh) << 4));
        const uint32_t r2 = ro + (uint32_t)(oq < 4 ? L_SPARE : A_XRING + 32768 - 4096);
        f32x4 sm[2];
#pragma unroll
        for (int v = 0; v < 4; ++v) {
            const f32x4 a0 = *(const f32x4*)(lds + v * 24576 + 8192 + ro) + *(const f32x4*)(lds + v * 4096 + r2);
            const f32x4 a1 = *(const f32x4*)(lds + v * 24576 + 16384 + ro) + *(const f32x4*)(lds + v * 24576 + ro);
            sm[0] = v == 0 ? a0 : sm[0] + a0;
            sm[1] = v == 0 ? a1 : sm[1] + a1;
        }
#pragma unroll
        for (int st = 0; st < 2; ++st) {
            u32x2 o2;
            o2[0] = pk_bf16(bf_lo(xres[st][0]) + sm[st][0], bf_hi(xres[st][0]) + sm[st][1]);
            o2[1] = pk_bf16(bf_lo(xres[st][1]) + sm[st][2], bf_hi(xres[st][1]) + sm[st][3]);
            st8_wt((const char*)out + st * 1024, ooff, o2);
        }
    }
    if (b == 0 && tid == 0) {
        const uint32_t e0 = (tg0 ^ 0x9e3779b9u) + 1u;
        st8_wt(ws + WS_EP, 0u, (u32x2){e0, tg1 ^ 0x7f4a7c15u});
    }
    TS_FLUSH(aux ? 2 : 0);
}

extern "C" void kernel_launch(void* const* d_in, const int* in_sizes, int n_in, void* d_out, int out_size, void* d_ws, size_t ws_size, hipStream_t stream) {
    fused_kernel<<<256, 512, 0, stream>>>((const uint16_t*)d_in[0], (const uint16_t*)d_in[1], (const uint16_t*)d_in[2], (const uint16_t*)d_in[4],
                                          (const uint16_t*)d_in[5], (const uint16_t*)d_in[6], (char*)d_ws, (uint16_t*)d_out);
}
```
